```python
import torch

def _softmax(x, axis=-1):
    return torch.softmax(x, dim=axis)
def _gelu(x):
    return torch.nn.functional.gelu(x, approximate="tanh")
def _silu(x):
    return torch.nn.functional.silu(x)
def _sigmoid(x):
    return torch.sigmoid(x)
def _layernorm(x, axis=-1, eps=1e-5):
    m = x.mean(dim=axis, keepdim=True)
    v = x.var(dim=axis, keepdim=True, unbiased=False)
    return (x - m) / torch.sqrt(v + eps)
def _scatter_add(tgt, idx, src, axis=0):
    return tgt.clone().index_add_(axis, idx, src)
def _segment_sum(src, seg, n):
    out = torch.zeros((n,) + src.shape[1:], dtype=src.dtype)
    return out.index_add_(0, seg, src)
def _pad(x, pw):
    flat = tuple(v for pair in reversed(pw) for v in pair)
    return torch.nn.functional.pad(x, flat)

kernel_name = 'concat_heads__mha_block__mha_block__prompt'

def setup_inputs():

    _w = (lambda a: (lambda t: t.to(torch.bfloat16) if t.is_floating_point() else t)(torch.as_tensor(a)))
    def _A_setup():
        B, S, D, H = 1, 2048, 640, 32
        return {
            "x": _w((lambda *s: torch.randn(*s, dtype=torch.bfloat16) * 0.2)(B, S, D)),
            "wqkv": _w((lambda *s: torch.randn(*s, dtype=torch.bfloat16) * 0.2)(D, 3 * D)),
            "wo": _w((lambda *s: torch.randn(*s, dtype=torch.bfloat16) * 0.05)(D, D)),
        }
    _A_in = _A_setup()

    def _B_setup():
        B, S, D, H = 1, 2048, 640, 32
        return {
            "x": _w((lambda *s: torch.randn(*s, dtype=torch.bfloat16) * 0.2)(B, S, D)),
            "wqkv": _w((lambda *s: torch.randn(*s, dtype=torch.bfloat16) * 0.2)(D, 3 * D)),
            "wo": _w((lambda *s: torch.randn(*s, dtype=torch.bfloat16) * 0.05)(D, D)),
        }
    _B_in = _B_setup()

    return {
        "A_x": _A_in["x"],
        "A_wqkv": _A_in["wqkv"],
        "A_wo": _A_in["wo"],
        "B_x": _B_in["x"],
        "B_wqkv": _B_in["wqkv"],
        "B_wo": _B_in["wo"],
    }

def reference(A_x, A_wqkv, A_wo, B_x, B_wqkv, B_wo):
    def _A_ref(x, wqkv, wo):
        B, S, D = x.shape
        H = 32
        Dh = D // H
        qkv = (lambda x, *d: x.permute(*d))((x @ wqkv).reshape(B, S, 3, H, Dh), 2, 0, 3, 1, 4)
        q, k, v = qkv[0], qkv[1], qkv[2]
        scores = torch.einsum("bhqd,bhkd->bhqk", q, k) * (Dh ** -0.5)
        p = _softmax(scores, axis=-1)
        o = torch.einsum("bhqk,bhkd->bhqd", p, v)
        return (lambda x, *d: x.permute(*d))(o, 0, 2, 1, 3).reshape(B, S, D) @ wo

    def _B_ref(x, wqkv, wo):
        B, S, D = x.shape
        H = 32
        Dh = D // H
        qkv = (lambda x, *d: x.permute(*d))((x @ wqkv).reshape(B, S, 3, H, Dh), 2, 0, 3, 1, 4)
        q, k, v = qkv[0], qkv[1], qkv[2]
        scores = torch.einsum("bhqd,bhkd->bhqk", q, k) * (Dh ** -0.5)
        p = _softmax(scores, axis=-1)
        o = torch.einsum("bhqk,bhkd->bhqd", p, v)
        return (lambda x, *d: x.permute(*d))(o, 0, 2, 1, 3).reshape(B, S, D) @ wo

    a = _A_ref(A_x, A_wqkv, A_wo)
    b = _B_ref(B_x, B_wqkv, B_wo)
    return torch.concatenate([a, b], axis=-1)
```

```cpp
#include <hip/hip_runtime.h>
#include <hip/hip_bf16.h>
#include <stdint.h>

typedef float f32x16 __attribute__((ext_vector_type(16)));
typedef float f32x4 __attribute__((ext_vector_type(4)));
typedef float f32x2 __attribute__((ext_vector_type(2)));
typedef __bf16 bf16x2 __attribute__((ext_vector_type(2)));
typedef __bf16 bf16x4 __attribute__((ext_vector_type(4)));
typedef __bf16 bf16x8 __attribute__((ext_vector_type(8)));
typedef short s16x4 __attribute__((ext_vector_type(4)));
typedef unsigned u32x2 __attribute__((ext_vector_type(2)));
typedef unsigned u32x4 __attribute__((ext_vector_type(4)));
typedef unsigned u32x8 __attribute__((ext_vector_type(8)));

#define SEQ 2048
#define DM 640
#define QIMG_HEAD 81920
#define KVIMG_HEAD 163840
#define OIMG_B (80 * 32768)
#define WS_Q 0
#define WS_KV (64 * QIMG_HEAD)
#define WS_O (WS_KV + 64 * KVIMG_HEAD)
#define QSCALE 0.32259442284f

#define ATTN_ASM \
  "s_mov_b64 s[40:41], %[img]\n" \
  "s_mov_b64 s[42:43], %[q]\n" \
  "s_mov_b64 s[44:45], %[qt]\n" \
  "s_mov_b32 s48, 0xfffff\n" \
  "s_mov_b32 s49, 0xfffff\n" \
  "s_mov_b32 s50, 0\n" \
  "s_mov_b32 s51, -1\n" \
  "v_mbcnt_lo_u32_b32 v193, -1, 0\n" \
  "v_mbcnt_hi_u32_b32 v193, -1, v193\n" \
  "v_lshlrev_b32 v192, 4, v193\n" \
  "s_mul_i32 s47, %[qg], 0x1400\n" \
  "s_add_u32 s40, s40, s47\n" \
  "s_addc_u32 s41, s41, 0\n" \
  "s_add_u32 m0, %[lds], s47\n" \
  "s_nop 0\n" \
  "global_load_lds_dwordx4 v192, s[40:41] offset:0\n" \
  "global_load_lds_dwordx4 v192, s[40:41] offset:1024\n" \
  "global_load_lds_dwordx4 v192, s[40:41] offset:2048\n" \
  "global_load_lds_dwordx4 v192, s[40:41] offset:3072\n" \
  "s_add_u32 m0, m0, 0x1000\n" \
  "s_add_u32 s40, s40, 0x1000\n" \
  "s_addc_u32 s41, s41, 0\n" \
  "global_load_lds_dwordx4 v192, s[40:41] offset:0\n" \
  "global_load_dwordx4 v[128:131], v192, s[42:43] offset:0\n" \
  "global_load_dwordx4 v[132:135], v192, s[42:43] offset:1024\n" \
  "global_load_dwordx4 v[136:139], v192, s[42:43] offset:2048\n" \
  "global_load_dwordx4 v[140:143], v192, s[42:43] offset:3072\n" \
  "v_and_b32 v190, 31, v193\n" \
  "v_lshlrev_b32 v191, 3, v190\n" \
  "global_load_dwordx2 v[144:145], v191, s[44:45] offset:0\n" \
  "global_load_dwordx2 v[146:147], v191, s[44:45] offset:256\n" \
  "global_load_dwordx2 v[148:149], v191, s[44:45] offset:512\n" \
  "global_load_dwordx2 v[150:151], v191, s[44:45] offset:768\n" \
  "s_mov_b64 s[40:41], %[img]\n" \
  "s_mul_i32 s47, %[qg], 0x3c00\n" \
  "s_add_u32 s47, s47, 0x5000\n" \
  "s_add_u32 s40, s40, s47\n" \
  "s_addc_u32 s41, s41, 0\n" \
  "s_add_u32 m0, %[lds], s47\n" \
  "v_add_u32 v189, %[lds], v192\n" \
  "v_lshrrev_b32 v191, 5, v193\n" \
  "v_mul_u32_u24 v191, 0x140, v191\n" \
  "v_lshl_add_u32 v191, v190, 4, v191\n" \
  "v_add_u32 v191, %[lds], v191\n" \
  "v_lshlrev_b32 v190, 3, v193\n" \
  "v_add_u32 v190, %[lds], v190\n" \
  "v_mov_b32 v188, 0xdddddddd\n" \
  "v_mov_b32 v64, 0\n" \
  "v_mov_b32 v65, 0\n" \
  "v_mov_b32 v66, 0\n" \
  "v_mov_b32 v67, 0\n" \
  "v_mov_b32 v68, 0\n" \
  "v_mov_b32 v69, 0\n" \
  "v_mov_b32 v70, 0\n" \
  "v_mov_b32 v71, 0\n" \
  "v_mov_b32 v72, 0\n" \
  "v_mov_b32 v73, 0\n" \
  "v_mov_b32 v74, 0\n" \
  "v_mov_b32 v75, 0\n" \
  "v_mov_b32 v76, 0\n" \
  "v_mov_b32 v77, 0\n" \
  "v_mov_b32 v78, 0\n" \
  "v_mov_b32 v79, 0\n" \
  "v_mov_b32 v80, 0\n" \
  "v_mov_b32 v81, 0\n" \
  "v_mov_b32 v82, 0\n" \
  "v_mov_b32 v83, 0\n" \
  "v_mov_b32 v84, 0\n" \
  "v_mov_b32 v85, 0\n" \
  "v_mov_b32 v86, 0\n" \
  "v_mov_b32 v87, 0\n" \
  "v_mov_b32 v88, 0\n" \
  "v_mov_b32 v89, 0\n" \
  "v_mov_b32 v90, 0\n" \
  "v_mov_b32 v91, 0\n" \
  "v_mov_b32 v92, 0\n" \
  "v_mov_b32 v93, 0\n" \
  "v_mov_b32 v94, 0\n" \
  "v_mov_b32 v95, 0\n" \
  "v_mov_b32 v96, 0\n" \
  "v_mov_b32 v97, 0\n" \
  "v_mov_b32 v98, 0\n" \
  "v_mov_b32 v99, 0\n" \
  "v_mov_b32 v100, 0\n" \
  "v_mov_b32 v101, 0\n" \
  "v_mov_b32 v102, 0\n" \
  "v_mov_b32 v103, 0\n" \
  "v_mov_b32 v104, 0\n" \
  "v_mov_b32 v105, 0\n" \
  "v_mov_b32 v106, 0\n" \
  "v_mov_b32 v107, 0\n" \
  "v_mov_b32 v108, 0\n" \
  "v_mov_b32 v109, 0\n" \
  "v_mov_b32 v110, 0\n" \
  "v_mov_b32 v111, 0\n" \
  "v_mov_b32 v112, 0\n" \
  "v_mov_b32 v113, 0\n" \
  "v_mov_b32 v114, 0\n" \
  "v_mov_b32 v115, 0\n" \
  "v_mov_b32 v116, 0\n" \
  "v_mov_b32 v117, 0\n" \
  "v_mov_b32 v118, 0\n" \
  "v_mov_b32 v119, 0\n" \
  "v_mov_b32 v120, 0\n" \
  "v_mov_b32 v121, 0\n" \
  "v_mov_b32 v122, 0\n" \
  "v_mov_b32 v123, 0\n" \
  "v_mov_b32 v124, 0\n" \
  "v_mov_b32 v125, 0\n" \
  "v_mov_b32 v126, 0\n" \
  "v_mov_b32 v127, 0\n" \
  "v_mov_b32 v172, 0x3f803f80\n" \
  "v_mov_b32 v173, 0x3f803f80\n" \
  "v_mov_b32 v174, 0x3f803f80\n" \
  "v_mov_b32 v175, 0x3f803f80\n" \
  "v_mov_b32 v176, 0x3f803f80\n" \
  "v_mov_b32 v177, 0x3f803f80\n" \
  "v_mov_b32 v178, 0x3f803f80\n" \
  "v_mov_b32 v179, 0x3f803f80\n" \
  "v_mov_b32 v180, 0x3f803f80\n" \
  "v_mov_b32 v181, 0x3f803f80\n" \
  "v_mov_b32 v182, 0x3f803f80\n" \
  "v_mov_b32 v183, 0x3f803f80\n" \
  "v_mov_b32 v184, 0x3f803f80\n" \
  "v_mov_b32 v185, 0x3f803f80\n" \
  "v_mov_b32 v186, 0x3f803f80\n" \
  "v_mov_b32 v187, 0x3f803f80\n" \
  "s_mov_b32 s46, 1\n" \
  "s_waitcnt vmcnt(0)\n" \
  "s_barrier\n" \
  "ds_read_b128 v[160:163], v189 offset:0\n" \
  "ds_read_b64 v[168:169], v190 offset:4096\n" \
  "s_mov_b64 exec, s[48:49]\n" \
  "ds_read_b128 v[172:175], v191 offset:5120\n" \
  "ds_read_b128 v[176:179], v191 offset:5760\n" \
  "s_mov_b64 exec, -1\n" \
  "v_cndmask_b32 v152, 0, v144, s[50:51]\n" \
  "v_cndmask_b32 v144, v144, 0, s[50:51]\n" \
  "v_cndmask_b32 v153, 0, v145, s[50:51]\n" \
  "v_cndmask_b32 v145, v145, 0, s[50:51]\n" \
  "v_cndmask_b32 v154, 0, v146, s[50:51]\n" \
  "v_cndmask_b32 v146, v146, 0, s[50:51]\n" \
  "v_cndmask_b32 v155, 0, v147, s[50:51]\n" \
  "v_cndmask_b32 v147, v147, 0, s[50:51]\n" \
  "v_cndmask_b32 v156, 0, v148, s[50:51]\n" \
  "v_cndmask_b32 v148, v148, 0, s[50:51]\n" \
  "v_cndmask_b32 v157, 0, v149, s[50:51]\n" \
  "v_cndmask_b32 v149, v149, 0, s[50:51]\n" \
  "v_cndmask_b32 v158, 0, v150, s[50:51]\n" \
  "v_cndmask_b32 v150, v150, 0, s[50:51]\n" \
  "v_cndmask_b32 v159, 0, v151, s[50:51]\n" \
  "v_cndmask_b32 v151, v151, 0, s[50:51]\n" \
  "s_waitcnt lgkmcnt(0)\n" \
  "s_nop 1\n" \
  "v_mfma_f32_32x32x16_bf16 v[0:15], v[160:163], v[128:131], 0\n" \
  "v_mfma_f32_32x32x16_bf16 v[16:31], v[160:163], v[132:135], 0\n" \
  "v_mfma_f32_32x32x8_bf16 v[0:15], v[168:169], v[144:145], v[0:15]\n" \
  "v_mfma_f32_32x32x8_bf16 v[16:31], v[168:169], v[146:147], v[16:31]\n" \
  "s_nop 15\n" \
  "v_exp_f32 v0, v0\n" \
  "v_exp_f32 v1, v1\n" \
  "ds_read_b128 v[164:167], v189 offset:1024\n" \
  "v_exp_f32 v2, v2\n" \
  "v_exp_f32 v3, v3\n" \
  "v_exp_f32 v4, v4\n" \
  "v_exp_f32 v5, v5\n" \
  "v_mfma_f32_32x32x16_bf16 v[32:47], v[160:163], v[136:139], 0\n" \
  "ds_read_b64 v[170:171], v190 offset:4608\n" \
  "v_exp_f32 v6, v6\n" \
  "v_exp_f32 v7, v7\n" \
  "v_exp_f32 v8, v8\n" \
  "v_exp_f32 v9, v9\n" \
  "v_smfmac_f32_32x32x32_bf16 v[64:79], v[172:175], v[0:7], v188\n" \
  "global_load_lds_dwordx4 v192, s[40:41] offset:0\n" \
  "v_exp_f32 v10, v10\n" \
  "v_exp_f32 v11, v11\n" \
  "v_exp_f32 v12, v12\n" \
  "v_exp_f32 v13, v13\n" \
  "v_mfma_f32_32x32x8_bf16 v[32:47], v[168:169], v[148:149], v[32:47]\n" \
  "v_exp_f32 v14, v14\n" \
  "v_exp_f32 v15, v15\n" \
  "v_exp_f32 v16, v16\n" \
  "v_exp_f32 v17, v17\n" \
  "v_smfmac_f32_32x32x32_bf16 v[64:79], v[176:179], v[8:15], v188\n" \
  "s_mov_b64 exec, s[48:49]\n" \
  "ds_read_b128 v[180:183], v191 offset:6400\n" \
  "ds_read_b128 v[184:187], v191 offset:7040\n" \
  "s_mov_b64 exec, -1\n" \
  "v_exp_f32 v18, v18\n" \
  "v_exp_f32 v19, v19\n" \
  "v_exp_f32 v20, v20\n" \
  "v_exp_f32 v21, v21\n" \
  "v_mfma_f32_32x32x16_bf16 v[48:63], v[160:163], v[140:143], 0\n" \
  "v_exp_f32 v22, v22\n" \
  "v_exp_f32 v23, v23\n" \
  "v_exp_f32 v24, v24\n" \
  "v_exp_f32 v25, v25\n" \
  "v_smfmac_f32_32x32x32_bf16 v[80:95], v[172:175], v[16:23], v188\n" \
  "global_load_lds_dwordx4 v192, s[40:41] offset:1024\n" \
  "v_exp_f32 v26, v26\n" \
  "v_exp_f32 v27, v27\n" \
  "v_exp_f32 v28, v28\n" \
  "v_exp_f32 v29, v29\n" \
  "v_mfma_f32_32x32x8_bf16 v[48:63], v[168:169], v[150:151], v[48:63]\n" \
  "v_exp_f32 v30, v30\n" \
  "v_exp_f32 v31, v31\n" \
  "v_exp_f32 v32, v32\n" \
  "v_exp_f32 v33, v33\n" \
  "v_smfmac_f32_32x32x32_bf16 v[80:95], v[176:179], v[24:31], v188\n" \
  "v_exp_f32 v34, v34\n" \
  "v_exp_f32 v35, v35\n" \
  "v_exp_f32 v36, v36\n" \
  "v_exp_f32 v37, v37\n" \
  "s_waitcnt lgkmcnt(0)\n" \
  "v_mfma_f32_32x32x16_bf16 v[0:15], v[164:167], v[128:131], 0\n" \
  "v_exp_f32 v38, v38\n" \
  "v_exp_f32 v39, v39\n" \
  "v_exp_f32 v40, v40\n" \
  "v_exp_f32 v41, v41\n" \
  "v_smfmac_f32_32x32x32_bf16 v[96:111], v[172:175], v[32:39], v188\n" \
  "global_load_lds_dwordx4 v192, s[40:41] offset:2048\n" \
  "v_exp_f32 v42, v42\n" \
  "v_exp_f32 v43, v43\n" \
  "v_exp_f32 v44, v44\n" \
  "v_exp_f32 v45, v45\n" \
  "v_mfma_f32_32x32x8_bf16 v[0:15], v[168:169], v[152:153], v[0:15]\n" \
  "v_exp_f32 v46, v46\n" \
  "v_exp_f32 v47, v47\n" \
  "v_exp_f32 v48, v48\n" \
  "v_exp_f32 v49, v49\n" \
  "v_smfmac_f32_32x32x32_bf16 v[96:111], v[176:179], v[40:47], v188\n" \
  "v_exp_f32 v50, v50\n" \
  "v_exp_f32 v51, v51\n" \
  "v_exp_f32 v52, v52\n" \
  "v_exp_f32 v53, v53\n" \
  "v_mfma_f32_32x32x16_bf16 v[16:31], v[164:167], v[132:135], 0\n" \
  "v_exp_f32 v54, v54\n" \
  "v_exp_f32 v55, v55\n" \
  "v_exp_f32 v56, v56\n" \
  "v_exp_f32 v57, v57\n" \
  "v_smfmac_f32_32x32x32_bf16 v[112:127], v[172:175], v[48:55], v188\n" \
  "global_load_lds_dwordx4 v192, s[40:41] offset:3072\n" \
  "v_exp_f32 v58, v58\n" \
  "v_exp_f32 v59, v59\n" \
  "v_exp_f32 v60, v60\n" \
  "v_exp_f32 v61, v61\n" \
  "v_mfma_f32_32x32x8_bf16 v[16:31], v[168:169], v[154:155], v[16:31]\n" \
  "v_exp_f32 v62, v62\n" \
  "v_exp_f32 v63, v63\n" \
  "v_exp_f32 v0, v0\n" \
  "v_exp_f32 v1, v1\n" \
  "v_smfmac_f32_32x32x32_bf16 v[112:127], v[176:179], v[56:63], v188\n" \
  "ds_read_b128 v[160:163], v189 offset:2048\n" \
  "v_exp_f32 v2, v2\n" \
  "v_exp_f32 v3, v3\n" \
  "v_exp_f32 v4, v4\n" \
  "v_exp_f32 v5, v5\n" \
  "v_mfma_f32_32x32x16_bf16 v[32:47], v[164:167], v[136:139], 0\n" \
  "s_add_u32 m0, m0, 0x1000\n" \
  "s_add_u32 s40, s40, 0x1000\n" \
  "s_addc_u32 s41, s41, 0\n" \
  "v_exp_f32 v6, v6\n" \
  "v_exp_f32 v7, v7\n" \
  "v_exp_f32 v8, v8\n" \
  "v_exp_f32 v9, v9\n" \
  "v_smfmac_f32_32x32x32_bf16 v[64:79], v[180:183], v[0:7], v188\n" \
  "global_load_lds_dwordx4 v192, s[40:41] offset:0\n" \
  "v_exp_f32 v10, v10\n" \
  "v_exp_f32 v11, v11\n" \
  "v_exp_f32 v12, v12\n" \
  "v_exp_f32 v13, v13\n" \
  "v_mfma_f32_32x32x8_bf16 v[32:47], v[168:169], v[156:157], v[32:47]\n" \
  "v_exp_f32 v14, v14\n" \
  "v_exp_f32 v15, v15\n" \
  "v_exp_f32 v16, v16\n" \
  "v_exp_f32 v17, v17\n" \
  "v_smfmac_f32_32x32x32_bf16 v[64:79], v[184:187], v[8:15], v188\n" \
  "s_mov_b64 exec, s[48:49]\n" \
  "ds_read_b128 v[172:175], v191 offset:7680\n" \
  "ds_read_b128 v[176:179], v191 offset:8320\n" \
  "s_mov_b64 exec, -1\n" \
  "v_exp_f32 v18, v18\n" \
  "v_exp_f32 v19, v19\n" \
  "v_exp_f32 v20, v20\n" \
  "v_exp_f32 v21, v21\n" \
  "v_mfma_f32_32x32x16_bf16 v[48:63], v[164:167], v[140:143], 0\n" \
  "v_exp_f32 v22, v22\n" \
  "v_exp_f32 v23, v23\n" \
  "v_exp_f32 v24, v24\n" \
  "v_exp_f32 v25, v25\n" \
  "v_smfmac_f32_32x32x32_bf16 v[80:95], v[180:183], v[16:23], v188\n" \
  "global_load_lds_dwordx4 v192, s[40:41] offset:1024\n" \
  "v_exp_f32 v26, v26\n" \
  "v_exp_f32 v27, v27\n" \
  "v_exp_f32 v28, v28\n" \
  "v_exp_f32 v29, v29\n" \
  "v_mfma_f32_32x32x8_bf16 v[48:63], v[168:169], v[158:159], v[48:63]\n" \
  "v_exp_f32 v30, v30\n" \
  "v_exp_f32 v31, v31\n" \
  "v_exp_f32 v32, v32\n" \
  "v_exp_f32 v33, v33\n" \
  "v_smfmac_f32_32x32x32_bf16 v[80:95], v[184:187], v[24:31], v188\n" \
  "v_exp_f32 v34, v34\n" \
  "v_exp_f32 v35, v35\n" \
  "v_exp_f32 v36, v36\n" \
  "v_exp_f32 v37, v37\n" \
  "s_waitcnt lgkmcnt(0)\n" \
  "v_mfma_f32_32x32x16_bf16 v[0:15], v[160:163], v[128:131], 0\n" \
  "v_exp_f32 v38, v38\n" \
  "v_exp_f32 v39, v39\n" \
  "v_exp_f32 v40, v40\n" \
  "v_exp_f32 v41, v41\n" \
  "v_smfmac_f32_32x32x32_bf16 v[96:111], v[180:183], v[32:39], v188\n" \
  "global_load_lds_dwordx4 v192, s[40:41] offset:2048\n" \
  "v_exp_f32 v42, v42\n" \
  "v_exp_f32 v43, v43\n" \
  "v_exp_f32 v44, v44\n" \
  "v_exp_f32 v45, v45\n" \
  "v_mfma_f32_32x32x8_bf16 v[0:15], v[170:171], v[144:145], v[0:15]\n" \
  "v_exp_f32 v46, v46\n" \
  "v_exp_f32 v47, v47\n" \
  "v_exp_f32 v48, v48\n" \
  "v_exp_f32 v49, v49\n" \
  "v_smfmac_f32_32x32x32_bf16 v[96:111], v[184:187], v[40:47], v188\n" \
  "v_exp_f32 v50, v50\n" \
  "v_exp_f32 v51, v51\n" \
  "v_exp_f32 v52, v52\n" \
  "v_exp_f32 v53, v53\n" \
  "v_mfma_f32_32x32x16_bf16 v[16:31], v[160:163], v[132:135], 0\n" \
  "v_exp_f32 v54, v54\n" \
  "v_exp_f32 v55, v55\n" \
  "v_exp_f32 v56, v56\n" \
  "v_exp_f32 v57, v57\n" \
  "v_smfmac_f32_32x32x32_bf16 v[112:127], v[180:183], v[48:55], v188\n" \
  "global_load_lds_dwordx4 v192, s[40:41] offset:3072\n" \
  "v_exp_f32 v58, v58\n" \
  "v_exp_f32 v59, v59\n" \
  "v_exp_f32 v60, v60\n" \
  "v_exp_f32 v61, v61\n" \
  "v_mfma_f32_32x32x8_bf16 v[16:31], v[170:171], v[146:147], v[16:31]\n" \
  "v_exp_f32 v62, v62\n" \
  "v_exp_f32 v63, v63\n" \
  "v_exp_f32 v0, v0\n" \
  "v_exp_f32 v1, v1\n" \
  "v_smfmac_f32_32x32x32_bf16 v[112:127], v[184:187], v[56:63], v188\n" \
  "ds_read_b128 v[164:167], v189 offset:3072\n" \
  "v_exp_f32 v2, v2\n" \
  "v_exp_f32 v3, v3\n" \
  "v_exp_f32 v4, v4\n" \
  "v_exp_f32 v5, v5\n" \
  "v_mfma_f32_32x32x16_bf16 v[32:47], v[160:163], v[136:139], 0\n" \
  "s_add_u32 m0, m0, 0x1000\n" \
  "s_add_u32 s40, s40, 0x1000\n" \
  "s_addc_u32 s41, s41, 0\n" \
  "v_exp_f32 v6, v6\n" \
  "v_exp_f32 v7, v7\n" \
  "v_exp_f32 v8, v8\n" \
  "v_exp_f32 v9, v9\n" \
  "v_smfmac_f32_32x32x32_bf16 v[64:79], v[172:175], v[0:7], v188\n" \
  "global_load_lds_dwordx4 v192, s[40:41] offset:0\n" \
  "v_exp_f32 v10, v10\n" \
  "v_exp_f32 v11, v11\n" \
  "v_exp_f32 v12, v12\n" \
  "v_exp_f32 v13, v13\n" \
  "v_mfma_f32_32x32x8_bf16 v[32:47], v[170:171], v[148:149], v[32:47]\n" \
  "v_exp_f32 v14, v14\n" \
  "v_exp_f32 v15, v15\n" \
  "v_exp_f32 v16, v16\n" \
  "v_exp_f32 v17, v17\n" \
  "v_smfmac_f32_32x32x32_bf16 v[64:79], v[176:179], v[8:15], v188\n" \
  "s_mov_b64 exec, s[48:49]\n" \
  "ds_read_b128 v[180:183], v191 offset:8960\n" \
  "ds_read_b128 v[184:187], v191 offset:9600\n" \
  "s_mov_b64 exec, -1\n" \
  "v_exp_f32 v18, v18\n" \
  "v_exp_f32 v19, v19\n" \
  "v_exp_f32 v20, v20\n" \
  "v_exp_f32 v21, v21\n" \
  "v_mfma_f32_32x32x16_bf16 v[48:63], v[160:163], v[140:143], 0\n" \
  "v_exp_f32 v22, v22\n" \
  "v_exp_f32 v23, v23\n" \
  "v_exp_f32 v24, v24\n" \
  "v_exp_f32 v25, v25\n" \
  "v_smfmac_f32_32x32x32_bf16 v[80:95], v[172:175], v[16:23], v188\n" \
  "global_load_lds_dwordx4 v192, s[40:41] offset:1024\n" \
  "v_exp_f32 v26, v26\n" \
  "v_exp_f32 v27, v27\n" \
  "v_exp_f32 v28, v28\n" \
  "v_exp_f32 v29, v29\n" \
  "v_mfma_f32_32x32x8_bf16 v[48:63], v[170:171], v[150:151], v[48:63]\n" \
  "v_exp_f32 v30, v30\n" \
  "v_exp_f32 v31, v31\n" \
  "v_exp_f32 v32, v32\n" \
  "v_exp_f32 v33, v33\n" \
  "v_smfmac_f32_32x32x32_bf16 v[80:95], v[176:179], v[24:31], v188\n" \
  "v_exp_f32 v34, v34\n" \
  "v_exp_f32 v35, v35\n" \
  "v_exp_f32 v36, v36\n" \
  "v_exp_f32 v37, v37\n" \
  "s_waitcnt lgkmcnt(0)\n" \
  "v_mfma_f32_32x32x16_bf16 v[0:15], v[164:167], v[128:131], 0\n" \
  "v_exp_f32 v38, v38\n" \
  "v_exp_f32 v39, v39\n" \
  "v_exp_f32 v40, v40\n" \
  "v_exp_f32 v41, v41\n" \
  "v_smfmac_f32_32x32x32_bf16 v[96:111], v[172:175], v[32:39], v188\n" \
  "global_load_lds_dwordx4 v192, s[40:41] offset:2048\n" \
  "v_exp_f32 v42, v42\n" \
  "v_exp_f32 v43, v43\n" \
  "v_exp_f32 v44, v44\n" \
  "v_exp_f32 v45, v45\n" \
  "v_mfma_f32_32x32x8_bf16 v[0:15], v[170:171], v[152:153], v[0:15]\n" \
  "v_exp_f32 v46, v46\n" \
  "v_exp_f32 v47, v47\n" \
  "v_exp_f32 v48, v48\n" \
  "v_exp_f32 v49, v49\n" \
  "v_smfmac_f32_32x32x32_bf16 v[96:111], v[176:179], v[40:47], v188\n" \
  "v_exp_f32 v50, v50\n" \
  "v_exp_f32 v51, v51\n" \
  "v_exp_f32 v52, v52\n" \
  "v_exp_f32 v53, v53\n" \
  "v_mfma_f32_32x32x16_bf16 v[16:31], v[164:167], v[132:135], 0\n" \
  "v_exp_f32 v54, v54\n" \
  "v_exp_f32 v55, v55\n" \
  "v_exp_f32 v56, v56\n" \
  "v_exp_f32 v57, v57\n" \
  "v_smfmac_f32_32x32x32_bf16 v[112:127], v[172:175], v[48:55], v188\n" \
  "global_load_lds_dwordx4 v192, s[40:41] offset:3072\n" \
  "v_exp_f32 v58, v58\n" \
  "v_exp_f32 v59, v59\n" \
  "v_exp_f32 v60, v60\n" \
  "v_exp_f32 v61, v61\n" \
  "v_mfma_f32_32x32x8_bf16 v[16:31], v[170:171], v[154:155], v[16:31]\n" \
  "v_exp_f32 v62, v62\n" \
  "v_exp_f32 v63, v63\n" \
  "v_exp_f32 v0, v0\n" \
  "v_exp_f32 v1, v1\n" \
  "v_smfmac_f32_32x32x32_bf16 v[112:127], v[176:179], v[56:63], v188\n" \
  "ds_read_b128 v[160:163], v189 offset:10240\n" \
  "v_exp_f32 v2, v2\n" \
  "v_exp_f32 v3, v3\n" \
  "v_exp_f32 v4, v4\n" \
  "v_exp_f32 v5, v5\n" \
  "v_mfma_f32_32x32x16_bf16 v[32:47], v[164:167], v[136:139], 0\n" \
  "ds_read_b64 v[168:169], v190 offset:14336\n" \
  "s_add_u32 m0, m0, 0x1000\n" \
  "s_add_u32 s40, s40, 0x1000\n" \
  "s_addc_u32 s41, s41, 0\n" \
  "v_exp_f32 v6, v6\n" \
  "v_exp_f32 v7, v7\n" \
  "v_exp_f32 v8, v8\n" \
  "v_exp_f32 v9, v9\n" \
  "v_smfmac_f32_32x32x32_bf16 v[64:79], v[180:183], v[0:7], v188\n" \
  "global_load_lds_dwordx4 v192, s[40:41] offset:0\n" \
  "v_exp_f32 v10, v10\n" \
  "v_exp_f32 v11, v11\n" \
  "v_exp_f32 v12, v12\n" \
  "v_exp_f32 v13, v13\n" \
  "v_mfma_f32_32x32x8_bf16 v[32:47], v[170:171], v[156:157], v[32:47]\n" \
  "v_exp_f32 v14, v14\n" \
  "v_exp_f32 v15, v15\n" \
  "v_exp_f32 v16, v16\n" \
  "v_exp_f32 v17, v17\n" \
  "v_smfmac_f32_32x32x32_bf16 v[64:79], v[184:187], v[8:15], v188\n" \
  "s_mov_b64 exec, s[48:49]\n" \
  "ds_read_b128 v[172:175], v191 offset:15360\n" \
  "ds_read_b128 v[176:179], v191 offset:16000\n" \
  "s_mov_b64 exec, -1\n" \
  "v_exp_f32 v18, v18\n" \
  "v_exp_f32 v19, v19\n" \
  "v_exp_f32 v20, v20\n" \
  "v_exp_f32 v21, v21\n" \
  "v_mfma_f32_32x32x16_bf16 v[48:63], v[164:167], v[140:143], 0\n" \
  "v_exp_f32 v22, v22\n" \
  "v_exp_f32 v23, v23\n" \
  "v_exp_f32 v24, v24\n" \
  "v_exp_f32 v25, v25\n" \
  "v_smfmac_f32_32x32x32_bf16 v[80:95], v[180:183], v[16:23], v188\n" \
  "global_load_lds_dwordx4 v192, s[40:41] offset:1024\n" \
  "v_exp_f32 v26, v26\n" \
  "v_exp_f32 v27, v27\n" \
  "v_exp_f32 v28, v28\n" \
  "v_exp_f32 v29, v29\n" \
  "v_mfma_f32_32x32x8_bf16 v[48:63], v[170:171], v[158:159], v[48:63]\n" \
  "v_exp_f32 v30, v30\n" \
  "v_exp_f32 v31, v31\n" \
  "v_exp_f32 v32, v32\n" \
  "v_exp_f32 v33, v33\n" \
  "v_smfmac_f32_32x32x32_bf16 v[80:95], v[184:187], v[24:31], v188\n" \
  "v_exp_f32 v34, v34\n" \
  "v_exp_f32 v35, v35\n" \
  "v_exp_f32 v36, v36\n" \
  "v_exp_f32 v37, v37\n" \
  "s_waitcnt lgkmcnt(0)\n" \
  "v_mfma_f32_32x32x16_bf16 v[0:15], v[160:163], v[128:131], 0\n" \
  "v_exp_f32 v38, v38\n" \
  "v_exp_f32 v39, v39\n" \
  "v_exp_f32 v40, v40\n" \
  "v_exp_f32 v41, v41\n" \
  "v_smfmac_f32_32x32x32_bf16 v[96:111], v[180:183], v[32:39], v188\n" \
  "global_load_lds_dwordx4 v192, s[40:41] offset:2048\n" \
  "v_exp_f32 v42, v42\n" \
  "v_exp_f32 v43, v43\n" \
  "v_exp_f32 v44, v44\n" \
  "v_exp_f32 v45, v45\n" \
  "v_mfma_f32_32x32x8_bf16 v[0:15], v[168:169], v[144:145], v[0:15]\n" \
  "v_exp_f32 v46, v46\n" \
  "v_exp_f32 v47, v47\n" \
  "v_exp_f32 v48, v48\n" \
  "v_exp_f32 v49, v49\n" \
  "v_smfmac_f32_32x32x32_bf16 v[96:111], v[184:187], v[40:47], v188\n" \
  "v_exp_f32 v50, v50\n" \
  "v_exp_f32 v51, v51\n" \
  "v_exp_f32 v52, v52\n" \
  "v_exp_f32 v53, v53\n" \
  "v_mfma_f32_32x32x16_bf16 v[16:31], v[160:163], v[132:135], 0\n" \
  "v_exp_f32 v54, v54\n" \
  "v_exp_f32 v55, v55\n" \
  "v_exp_f32 v56, v56\n" \
  "v_exp_f32 v57, v57\n" \
  "v_smfmac_f32_32x32x32_bf16 v[112:127], v[180:183], v[48:55], v188\n" \
  "v_add_u32 v189, 0x2800, v189\n" \
  "v_add_u32 v190, 0x2800, v190\n" \
  "v_add_u32 v191, 0x2800, v191\n" \
  "v_exp_f32 v58, v58\n" \
  "v_exp_f32 v59, v59\n" \
  "v_exp_f32 v60, v60\n" \
  "v_exp_f32 v61, v61\n" \
  "v_mfma_f32_32x32x8_bf16 v[16:31], v[168:169], v[146:147], v[16:31]\n" \
  "v_exp_f32 v62, v62\n" \
  "v_exp_f32 v63, v63\n" \
  "L_loop_%=:\n" \
  "v_exp_f32 v0, v0\n" \
  "v_exp_f32 v1, v1\n" \
  "v_smfmac_f32_32x32x32_bf16 v[112:127], v[184:187], v[56:63], v188\n" \
  "ds_read_b128 v[164:167], v189 offset:1024\n" \
  "v_exp_f32 v2, v2\n" \
  "v_exp_f32 v3, v3\n" \
  "v_exp_f32 v4, v4\n" \
  "v_exp_f32 v5, v5\n" \
  "v_mfma_f32_32x32x16_bf16 v[32:47], v[160:163], v[136:139], 0\n" \
  "ds_read_b64 v[170:171], v190 offset:4608\n" \
  "v_exp_f32 v6, v6\n" \
  "v_exp_f32 v7, v7\n" \
  "v_exp_f32 v8, v8\n" \
  "v_exp_f32 v9, v9\n" \
  "v_smfmac_f32_32x32x32_bf16 v[64:79], v[172:175], v[0:7], v188\n" \
  "v_exp_f32 v10, v10\n" \
  "v_exp_f32 v11, v11\n" \
  "v_exp_f32 v12, v12\n" \
  "v_exp_f32 v13, v13\n" \
  "v_mfma_f32_32x32x8_bf16 v[32:47], v[168:169], v[148:149], v[32:47]\n" \
  "v_exp_f32 v14, v14\n" \
  "v_exp_f32 v15, v15\n" \
  "v_exp_f32 v16, v16\n" \
  "v_exp_f32 v17, v17\n" \
  "v_smfmac_f32_32x32x32_bf16 v[64:79], v[176:179], v[8:15], v188\n" \
  "s_mov_b64 exec, s[48:49]\n" \
  "ds_read_b128 v[180:183], v191 offset:6400\n" \
  "ds_read_b128 v[184:187], v191 offset:7040\n" \
  "s_mov_b64 exec, -1\n" \
  "v_exp_f32 v18, v18\n" \
  "v_exp_f32 v19, v19\n" \
  "v_exp_f32 v20, v20\n" \
  "v_exp_f32 v21, v21\n" \
  "v_mfma_f32_32x32x16_bf16 v[48:63], v[160:163], v[140:143], 0\n" \
  "v_exp_f32 v22, v22\n" \
  "v_exp_f32 v23, v23\n" \
  "v_exp_f32 v24, v24\n" \
  "v_exp_f32 v25, v25\n" \
  "v_smfmac_f32_32x32x32_bf16 v[80:95], v[172:175], v[16:23], v188\n" \
  "v_exp_f32 v26, v26\n" \
  "v_exp_f32 v27, v27\n" \
  "v_exp_f32 v28, v28\n" \
  "v_exp_f32 v29, v29\n" \
  "v_mfma_f32_32x32x8_bf16 v[48:63], v[168:169], v[150:151], v[48:63]\n" \
  "v_exp_f32 v30, v30\n" \
  "v_exp_f32 v31, v31\n" \
  "v_exp_f32 v32, v32\n" \
  "v_exp_f32 v33, v33\n" \
  "v_smfmac_f32_32x32x32_bf16 v[80:95], v[176:179], v[24:31], v188\n" \
  "v_exp_f32 v34, v34\n" \
  "v_exp_f32 v35, v35\n" \
  "v_exp_f32 v36, v36\n" \
  "v_exp_f32 v37, v37\n" \
  "s_waitcnt lgkmcnt(0)\n" \
  "v_mfma_f32_32x32x16_bf16 v[0:15], v[164:167], v[128:131], 0\n" \
  "v_exp_f32 v38, v38\n" \
  "v_exp_f32 v39, v39\n" \
  "v_exp_f32 v40, v40\n" \
  "v_exp_f32 v41, v41\n" \
  "v_smfmac_f32_32x32x32_bf16 v[96:111], v[172:175], v[32:39], v188\n" \
  "v_exp_f32 v42, v42\n" \
  "v_exp_f32 v43, v43\n" \
  "v_exp_f32 v44, v44\n" \
  "v_exp_f32 v45, v45\n" \
  "v_mfma_f32_32x32x8_bf16 v[0:15], v[168:169], v[152:153], v[0:15]\n" \
  "v_exp_f32 v46, v46\n" \
  "v_exp_f32 v47, v47\n" \
  "v_exp_f32 v48, v48\n" \
  "v_exp_f32 v49, v49\n" \
  "v_smfmac_f32_32x32x32_bf16 v[96:111], v[176:179], v[40:47], v188\n" \
  "v_exp_f32 v50, v50\n" \
  "v_exp_f32 v51, v51\n" \
  "v_exp_f32 v52, v52\n" \
  "v_exp_f32 v53, v53\n" \
  "v_mfma_f32_32x32x16_bf16 v[16:31], v[164:167], v[132:135], 0\n" \
  "v_exp_f32 v54, v54\n" \
  "v_exp_f32 v55, v55\n" \
  "v_exp_f32 v56, v56\n" \
  "v_exp_f32 v57, v57\n" \
  "v_smfmac_f32_32x32x32_bf16 v[112:127], v[172:175], v[48:55], v188\n" \
  "v_exp_f32 v58, v58\n" \
  "v_exp_f32 v59, v59\n" \
  "v_exp_f32 v60, v60\n" \
  "v_exp_f32 v61, v61\n" \
  "v_mfma_f32_32x32x8_bf16 v[16:31], v[168:169], v[154:155], v[16:31]\n" \
  "v_exp_f32 v62, v62\n" \
  "v_exp_f32 v63, v63\n" \
  "v_exp_f32 v0, v0\n" \
  "v_exp_f32 v1, v1\n" \
  "v_smfmac_f32_32x32x32_bf16 v[112:127], v[176:179], v[56:63], v188\n" \
  "ds_read_b128 v[160:163], v189 offset:2048\n" \
  "v_exp_f32 v2, v2\n" \
  "v_exp_f32 v3, v3\n" \
  "v_exp_f32 v4, v4\n" \
  "v_exp_f32 v5, v5\n" \
  "v_mfma_f32_32x32x16_bf16 v[32:47], v[164:167], v[136:139], 0\n" \
  "v_exp_f32 v6, v6\n" \
  "v_exp_f32 v7, v7\n" \
  "v_exp_f32 v8, v8\n" \
  "v_exp_f32 v9, v9\n" \
  "v_smfmac_f32_32x32x32_bf16 v[64:79], v[180:183], v[0:7], v188\n" \
  "v_exp_f32 v10, v10\n" \
  "v_exp_f32 v11, v11\n" \
  "v_exp_f32 v12, v12\n" \
  "v_exp_f32 v13, v13\n" \
  "v_mfma_f32_32x32x8_bf16 v[32:47], v[168:169], v[156:157], v[32:47]\n" \
  "v_exp_f32 v14, v14\n" \
  "v_exp_f32 v15, v15\n" \
  "v_exp_f32 v16, v16\n" \
  "v_exp_f32 v17, v17\n" \
  "v_smfmac_f32_32x32x32_bf16 v[64:79], v[184:187], v[8:15], v188\n" \
  "s_mov_b64 exec, s[48:49]\n" \
  "ds_read_b128 v[172:175], v191 offset:7680\n" \
  "ds_read_b128 v[176:179], v191 offset:8320\n" \
  "s_mov_b64 exec, -1\n" \
  "v_exp_f32 v18, v18\n" \
  "v_exp_f32 v19, v19\n" \
  "v_exp_f32 v20, v20\n" \
  "v_exp_f32 v21, v21\n" \
  "v_mfma_f32_32x32x16_bf16 v[48:63], v[164:167], v[140:143], 0\n" \
  "v_exp_f32 v22, v22\n" \
  "v_exp_f32 v23, v23\n" \
  "v_exp_f32 v24, v24\n" \
  "v_exp_f32 v25, v25\n" \
  "v_smfmac_f32_32x32x32_bf16 v[80:95], v[180:183], v[16:23], v188\n" \
  "v_exp_f32 v26, v26\n" \
  "v_exp_f32 v27, v27\n" \
  "v_exp_f32 v28, v28\n" \
  "v_exp_f32 v29, v29\n" \
  "v_mfma_f32_32x32x8_bf16 v[48:63], v[168:169], v[158:159], v[48:63]\n" \
  "v_exp_f32 v30, v30\n" \
  "v_exp_f32 v31, v31\n" \
  "v_exp_f32 v32, v32\n" \
  "v_exp_f32 v33, v33\n" \
  "v_smfmac_f32_32x32x32_bf16 v[80:95], v[184:187], v[24:31], v188\n" \
  "v_exp_f32 v34, v34\n" \
  "v_exp_f32 v35, v35\n" \
  "v_exp_f32 v36, v36\n" \
  "v_exp_f32 v37, v37\n" \
  "s_waitcnt lgkmcnt(0)\n" \
  "v_mfma_f32_32x32x16_bf16 v[0:15], v[160:163], v[128:131], 0\n" \
  "v_exp_f32 v38, v38\n" \
  "v_exp_f32 v39, v39\n" \
  "v_exp_f32 v40, v40\n" \
  "v_exp_f32 v41, v41\n" \
  "v_smfmac_f32_32x32x32_bf16 v[96:111], v[180:183], v[32:39], v188\n" \
  "v_exp_f32 v42, v42\n" \
  "v_exp_f32 v43, v43\n" \
  "v_exp_f32 v44, v44\n" \
  "v_exp_f32 v45, v45\n" \
  "v_mfma_f32_32x32x8_bf16 v[0:15], v[170:171], v[144:145], v[0:15]\n" \
  "v_exp_f32 v46, v46\n" \
  "v_exp_f32 v47, v47\n" \
  "v_exp_f32 v48, v48\n" \
  "v_exp_f32 v49, v49\n" \
  "v_smfmac_f32_32x32x32_bf16 v[96:111], v[184:187], v[40:47], v188\n" \
  "v_exp_f32 v50, v50\n" \
  "v_exp_f32 v51, v51\n" \
  "v_exp_f32 v52, v52\n" \
  "v_exp_f32 v53, v53\n" \
  "v_mfma_f32_32x32x16_bf16 v[16:31], v[160:163], v[132:135], 0\n" \
  "v_exp_f32 v54, v54\n" \
  "v_exp_f32 v55, v55\n" \
  "v_exp_f32 v56, v56\n" \
  "v_exp_f32 v57, v57\n" \
  "v_smfmac_f32_32x32x32_bf16 v[112:127], v[180:183], v[48:55], v188\n" \
  "v_exp_f32 v58, v58\n" \
  "v_exp_f32 v59, v59\n" \
  "v_exp_f32 v60, v60\n" \
  "v_exp_f32 v61, v61\n" \
  "v_mfma_f32_32x32x8_bf16 v[16:31], v[170:171], v[146:147], v[16:31]\n" \
  "v_exp_f32 v62, v62\n" \
  "v_exp_f32 v63, v63\n" \
  "v_exp_f32 v0, v0\n" \
  "v_exp_f32 v1, v1\n" \
  "v_smfmac_f32_32x32x32_bf16 v[112:127], v[184:187], v[56:63], v188\n" \
  "ds_read_b128 v[164:167], v189 offset:3072\n" \
  "v_exp_f32 v2, v2\n" \
  "v_exp_f32 v3, v3\n" \
  "v_exp_f32 v4, v4\n" \
  "v_exp_f32 v5, v5\n" \
  "v_mfma_f32_32x32x16_bf16 v[32:47], v[160:163], v[136:139], 0\n" \
  "v_exp_f32 v6, v6\n" \
  "v_exp_f32 v7, v7\n" \
  "v_exp_f32 v8, v8\n" \
  "v_exp_f32 v9, v9\n" \
  "v_smfmac_f32_32x32x32_bf16 v[64:79], v[172:175], v[0:7], v188\n" \
  "v_exp_f32 v10, v10\n" \
  "v_exp_f32 v11, v11\n" \
  "v_exp_f32 v12, v12\n" \
  "v_exp_f32 v13, v13\n" \
  "v_mfma_f32_32x32x8_bf16 v[32:47], v[170:171], v[148:149], v[32:47]\n" \
  "v_exp_f32 v14, v14\n" \
  "v_exp_f32 v15, v15\n" \
  "v_exp_f32 v16, v16\n" \
  "v_exp_f32 v17, v17\n" \
  "v_smfmac_f32_32x32x32_bf16 v[64:79], v[176:179], v[8:15], v188\n" \
  "s_mov_b64 exec, s[48:49]\n" \
  "ds_read_b128 v[180:183], v191 offset:8960\n" \
  "ds_read_b128 v[184:187], v191 offset:9600\n" \
  "s_mov_b64 exec, -1\n" \
  "v_exp_f32 v18, v18\n" \
  "v_exp_f32 v19, v19\n" \
  "v_exp_f32 v20, v20\n" \
  "v_exp_f32 v21, v21\n" \
  "v_mfma_f32_32x32x16_bf16 v[48:63], v[160:163], v[140:143], 0\n" \
  "v_exp_f32 v22, v22\n" \
  "v_exp_f32 v23, v23\n" \
  "v_exp_f32 v24, v24\n" \
  "v_exp_f32 v25, v25\n" \
  "v_smfmac_f32_32x32x32_bf16 v[80:95], v[172:175], v[16:23], v188\n" \
  "v_exp_f32 v26, v26\n" \
  "v_exp_f32 v27, v27\n" \
  "v_exp_f32 v28, v28\n" \
  "v_exp_f32 v29, v29\n" \
  "v_mfma_f32_32x32x8_bf16 v[48:63], v[170:171], v[150:151], v[48:63]\n" \
  "v_exp_f32 v30, v30\n" \
  "v_exp_f32 v31, v31\n" \
  "v_exp_f32 v32, v32\n" \
  "v_exp_f32 v33, v33\n" \
  "v_smfmac_f32_32x32x32_bf16 v[80:95], v[176:179], v[24:31], v188\n" \
  "v_exp_f32 v34, v34\n" \
  "v_exp_f32 v35, v35\n" \
  "v_exp_f32 v36, v36\n" \
  "v_exp_f32 v37, v37\n" \
  "s_waitcnt lgkmcnt(0)\n" \
  "v_mfma_f32_32x32x16_bf16 v[0:15], v[164:167], v[128:131], 0\n" \
  "v_exp_f32 v38, v38\n" \
  "v_exp_f32 v39, v39\n" \
  "v_exp_f32 v40, v40\n" \
  "v_exp_f32 v41, v41\n" \
  "v_smfmac_f32_32x32x32_bf16 v[96:111], v[172:175], v[32:39], v188\n" \
  "v_exp_f32 v42, v42\n" \
  "v_exp_f32 v43, v43\n" \
  "v_exp_f32 v44, v44\n" \
  "v_exp_f32 v45, v45\n" \
  "v_mfma_f32_32x32x8_bf16 v[0:15], v[170:171], v[152:153], v[0:15]\n" \
  "v_exp_f32 v46, v46\n" \
  "v_exp_f32 v47, v47\n" \
  "v_exp_f32 v48, v48\n" \
  "v_exp_f32 v49, v49\n" \
  "v_smfmac_f32_32x32x32_bf16 v[96:111], v[176:179], v[40:47], v188\n" \
  "v_exp_f32 v50, v50\n" \
  "v_exp_f32 v51, v51\n" \
  "v_exp_f32 v52, v52\n" \
  "v_exp_f32 v53, v53\n" \
  "v_mfma_f32_32x32x16_bf16 v[16:31], v[164:167], v[132:135], 0\n" \
  "v_exp_f32 v54, v54\n" \
  "v_exp_f32 v55, v55\n" \
  "v_exp_f32 v56, v56\n" \
  "v_exp_f32 v57, v57\n" \
  "v_smfmac_f32_32x32x32_bf16 v[112:127], v[172:175], v[48:55], v188\n" \
  "v_exp_f32 v58, v58\n" \
  "v_exp_f32 v59, v59\n" \
  "v_exp_f32 v60, v60\n" \
  "v_exp_f32 v61, v61\n" \
  "v_mfma_f32_32x32x8_bf16 v[16:31], v[170:171], v[154:155], v[16:31]\n" \
  "v_exp_f32 v62, v62\n" \
  "v_exp_f32 v63, v63\n" \
  "s_cmp_lg_u32 s46, 1\n" \
  "s_cbranch_scc1 L_nobar_%=\n" \
  "s_waitcnt vmcnt(0)\n" \
  "s_barrier\n" \
  "L_nobar_%=:\n" \
  "v_exp_f32 v0, v0\n" \
  "v_exp_f32 v1, v1\n" \
  "v_smfmac_f32_32x32x32_bf16 v[112:127], v[176:179], v[56:63], v188\n" \
  "ds_read_b128 v[160:163], v189 offset:10240\n" \
  "v_exp_f32 v2, v2\n" \
  "v_exp_f32 v3, v3\n" \
  "v_exp_f32 v4, v4\n" \
  "v_exp_f32 v5, v5\n" \
  "v_mfma_f32_32x32x16_bf16 v[32:47], v[164:167], v[136:139], 0\n" \
  "ds_read_b64 v[168:169], v190 offset:14336\n" \
  "v_exp_f32 v6, v6\n" \
  "v_exp_f32 v7, v7\n" \
  "v_exp_f32 v8, v8\n" \
  "v_exp_f32 v9, v9\n" \
  "v_smfmac_f32_32x32x32_bf16 v[64:79], v[180:183], v[0:7], v188\n" \
  "v_exp_f32 v10, v10\n" \
  "v_exp_f32 v11, v11\n" \
  "v_exp_f32 v12, v12\n" \
  "v_exp_f32 v13, v13\n" \
  "v_mfma_f32_32x32x8_bf16 v[32:47], v[170:171], v[156:157], v[32:47]\n" \
  "v_exp_f32 v14, v14\n" \
  "v_exp_f32 v15, v15\n" \
  "v_exp_f32 v16, v16\n" \
  "v_exp_f32 v17, v17\n" \
  "v_smfmac_f32_32x32x32_bf16 v[64:79], v[184:187], v[8:15], v188\n" \
  "s_mov_b64 exec, s[48:49]\n" \
  "ds_read_b128 v[172:175], v191 offset:15360\n" \
  "ds_read_b128 v[176:179], v191 offset:16000\n" \
  "s_mov_b64 exec, -1\n" \
  "v_exp_f32 v18, v18\n" \
  "v_exp_f32 v19, v19\n" \
  "v_exp_f32 v20, v20\n" \
  "v_exp_f32 v21, v21\n" \
  "v_mfma_f32_32x32x16_bf16 v[48:63], v[164:167], v[140:143], 0\n" \
  "v_exp_f32 v22, v22\n" \
  "v_exp_f32 v23, v23\n" \
  "v_exp_f32 v24, v24\n" \
  "v_exp_f32 v25, v25\n" \
  "v_smfmac_f32_32x32x32_bf16 v[80:95], v[180:183], v[16:23], v188\n" \
  "v_exp_f32 v26, v26\n" \
  "v_exp_f32 v27, v27\n" \
  "v_exp_f32 v28, v28\n" \
  "v_exp_f32 v29, v29\n" \
  "v_mfma_f32_32x32x8_bf16 v[48:63], v[170:171], v[158:159], v[48:63]\n" \
  "v_exp_f32 v30, v30\n" \
  "v_exp_f32 v31, v31\n" \
  "v_exp_f32 v32, v32\n" \
  "v_exp_f32 v33, v33\n" \
  "v_smfmac_f32_32x32x32_bf16 v[80:95], v[184:187], v[24:31], v188\n" \
  "v_exp_f32 v34, v34\n" \
  "v_exp_f32 v35, v35\n" \
  "v_exp_f32 v36, v36\n" \
  "v_exp_f32 v37, v37\n" \
  "s_waitcnt lgkmcnt(0)\n" \
  "v_mfma_f32_32x32x16_bf16 v[0:15], v[160:163], v[128:131], 0\n" \
  "v_exp_f32 v38, v38\n" \
  "v_exp_f32 v39, v39\n" \
  "v_exp_f32 v40, v40\n" \
  "v_exp_f32 v41, v41\n" \
  "v_smfmac_f32_32x32x32_bf16 v[96:111], v[180:183], v[32:39], v188\n" \
  "v_exp_f32 v42, v42\n" \
  "v_exp_f32 v43, v43\n" \
  "v_exp_f32 v44, v44\n" \
  "v_exp_f32 v45, v45\n" \
  "v_mfma_f32_32x32x8_bf16 v[0:15], v[168:169], v[144:145], v[0:15]\n" \
  "v_exp_f32 v46, v46\n" \
  "v_exp_f32 v47, v47\n" \
  "v_exp_f32 v48, v48\n" \
  "v_exp_f32 v49, v49\n" \
  "v_smfmac_f32_32x32x32_bf16 v[96:111], v[184:187], v[40:47], v188\n" \
  "v_exp_f32 v50, v50\n" \
  "v_exp_f32 v51, v51\n" \
  "v_exp_f32 v52, v52\n" \
  "v_exp_f32 v53, v53\n" \
  "v_mfma_f32_32x32x16_bf16 v[16:31], v[160:163], v[132:135], 0\n" \
  "v_exp_f32 v54, v54\n" \
  "v_exp_f32 v55, v55\n" \
  "v_exp_f32 v56, v56\n" \
  "v_exp_f32 v57, v57\n" \
  "v_smfmac_f32_32x32x32_bf16 v[112:127], v[180:183], v[48:55], v188\n" \
  "v_add_u32 v189, 0x2800, v189\n" \
  "v_add_u32 v190, 0x2800, v190\n" \
  "v_add_u32 v191, 0x2800, v191\n" \
  "v_exp_f32 v58, v58\n" \
  "v_exp_f32 v59, v59\n" \
  "v_exp_f32 v60, v60\n" \
  "v_exp_f32 v61, v61\n" \
  "v_mfma_f32_32x32x8_bf16 v[16:31], v[168:169], v[146:147], v[16:31]\n" \
  "v_exp_f32 v62, v62\n" \
  "v_exp_f32 v63, v63\n" \
  "s_add_u32 s46, s46, 1\n" \
  "s_cmp_lg_u32 s46, 7\n" \
  "s_cbranch_scc1 L_loop_%=\n" \
  "v_exp_f32 v0, v0\n" \
  "v_exp_f32 v1, v1\n" \
  "v_smfmac_f32_32x32x32_bf16 v[112:127], v[184:187], v[56:63], v188\n" \
  "ds_read_b128 v[164:167], v189 offset:1024\n" \
  "v_exp_f32 v2, v2\n" \
  "v_exp_f32 v3, v3\n" \
  "v_exp_f32 v4, v4\n" \
  "v_exp_f32 v5, v5\n" \
  "v_mfma_f32_32x32x16_bf16 v[32:47], v[160:163], v[136:139], 0\n" \
  "ds_read_b64 v[170:171], v190 offset:4608\n" \
  "v_exp_f32 v6, v6\n" \
  "v_exp_f32 v7, v7\n" \
  "v_exp_f32 v8, v8\n" \
  "v_exp_f32 v9, v9\n" \
  "v_smfmac_f32_32x32x32_bf16 v[64:79], v[172:175], v[0:7], v188\n" \
  "v_exp_f32 v10, v10\n" \
  "v_exp_f32 v11, v11\n" \
  "v_exp_f32 v12, v12\n" \
  "v_exp_f32 v13, v13\n" \
  "v_mfma_f32_32x32x8_bf16 v[32:47], v[168:169], v[148:149], v[32:47]\n" \
  "v_exp_f32 v14, v14\n" \
  "v_exp_f32 v15, v15\n" \
  "v_exp_f32 v16, v16\n" \
  "v_exp_f32 v17, v17\n" \
  "v_smfmac_f32_32x32x32_bf16 v[64:79], v[176:179], v[8:15], v188\n" \
  "s_mov_b64 exec, s[48:49]\n" \
  "ds_read_b128 v[180:183], v191 offset:6400\n" \
  "ds_read_b128 v[184:187], v191 offset:7040\n" \
  "s_mov_b64 exec, -1\n" \
  "v_exp_f32 v18, v18\n" \
  "v_exp_f32 v19, v19\n" \
  "v_exp_f32 v20, v20\n" \
  "v_exp_f32 v21, v21\n" \
  "v_mfma_f32_32x32x16_bf16 v[48:63], v[160:163], v[140:143], 0\n" \
  "v_exp_f32 v22, v22\n" \
  "v_exp_f32 v23, v23\n" \
  "v_exp_f32 v24, v24\n" \
  "v_exp_f32 v25, v25\n" \
  "v_smfmac_f32_32x32x32_bf16 v[80:95], v[172:175], v[16:23], v188\n" \
  "v_exp_f32 v26, v26\n" \
  "v_exp_f32 v27, v27\n" \
  "v_exp_f32 v28, v28\n" \
  "v_exp_f32 v29, v29\n" \
  "v_mfma_f32_32x32x8_bf16 v[48:63], v[168:169], v[150:151], v[48:63]\n" \
  "v_exp_f32 v30, v30\n" \
  "v_exp_f32 v31, v31\n" \
  "v_exp_f32 v32, v32\n" \
  "v_exp_f32 v33, v33\n" \
  "v_smfmac_f32_32x32x32_bf16 v[80:95], v[176:179], v[24:31], v188\n" \
  "v_exp_f32 v34, v34\n" \
  "v_exp_f32 v35, v35\n" \
  "v_exp_f32 v36, v36\n" \
  "v_exp_f32 v37, v37\n" \
  "s_waitcnt lgkmcnt(0)\n" \
  "v_mfma_f32_32x32x16_bf16 v[0:15], v[164:167], v[128:131], 0\n" \
  "v_exp_f32 v38, v38\n" \
  "v_exp_f32 v39, v39\n" \
  "v_exp_f32 v40, v40\n" \
  "v_exp_f32 v41, v41\n" \
  "v_smfmac_f32_32x32x32_bf16 v[96:111], v[172:175], v[32:39], v188\n" \
  "v_exp_f32 v42, v42\n" \
  "v_exp_f32 v43, v43\n" \
  "v_exp_f32 v44, v44\n" \
  "v_exp_f32 v45, v45\n" \
  "v_mfma_f32_32x32x8_bf16 v[0:15], v[168:169], v[152:153], v[0:15]\n" \
  "v_exp_f32 v46, v46\n" \
  "v_exp_f32 v47, v47\n" \
  "v_exp_f32 v48, v48\n" \
  "v_exp_f32 v49, v49\n" \
  "v_smfmac_f32_32x32x32_bf16 v[96:111], v[176:179], v[40:47], v188\n" \
  "v_exp_f32 v50, v50\n" \
  "v_exp_f32 v51, v51\n" \
  "v_exp_f32 v52, v52\n" \
  "v_exp_f32 v53, v53\n" \
  "v_mfma_f32_32x32x16_bf16 v[16:31], v[164:167], v[132:135], 0\n" \
  "v_exp_f32 v54, v54\n" \
  "v_exp_f32 v55, v55\n" \
  "v_exp_f32 v56, v56\n" \
  "v_exp_f32 v57, v57\n" \
  "v_smfmac_f32_32x32x32_bf16 v[112:127], v[172:175], v[48:55], v188\n" \
  "v_exp_f32 v58, v58\n" \
  "v_exp_f32 v59, v59\n" \
  "v_exp_f32 v60, v60\n" \
  "v_exp_f32 v61, v61\n" \
  "v_mfma_f32_32x32x8_bf16 v[16:31], v[168:169], v[154:155], v[16:31]\n" \
  "v_exp_f32 v62, v62\n" \
  "v_exp_f32 v63, v63\n" \
  "v_exp_f32 v0, v0\n" \
  "v_exp_f32 v1, v1\n" \
  "v_smfmac_f32_32x32x32_bf16 v[112:127], v[176:179], v[56:63], v188\n" \
  "ds_read_b128 v[160:163], v189 offset:2048\n" \
  "v_exp_f32 v2, v2\n" \
  "v_exp_f32 v3, v3\n" \
  "v_exp_f32 v4, v4\n" \
  "v_exp_f32 v5, v5\n" \
  "v_mfma_f32_32x32x16_bf16 v[32:47], v[164:167], v[136:139], 0\n" \
  "v_exp_f32 v6, v6\n" \
  "v_exp_f32 v7, v7\n" \
  "v_exp_f32 v8, v8\n" \
  "v_exp_f32 v9, v9\n" \
  "v_smfmac_f32_32x32x32_bf16 v[64:79], v[180:183], v[0:7], v188\n" \
  "v_exp_f32 v10, v10\n" \
  "v_exp_f32 v11, v11\n" \
  "v_exp_f32 v12, v12\n" \
  "v_exp_f32 v13, v13\n" \
  "v_mfma_f32_32x32x8_bf16 v[32:47], v[168:169], v[156:157], v[32:47]\n" \
  "v_exp_f32 v14, v14\n" \
  "v_exp_f32 v15, v15\n" \
  "v_exp_f32 v16, v16\n" \
  "v_exp_f32 v17, v17\n" \
  "v_smfmac_f32_32x32x32_bf16 v[64:79], v[184:187], v[8:15], v188\n" \
  "s_mov_b64 exec, s[48:49]\n" \
  "ds_read_b128 v[172:175], v191 offset:7680\n" \
  "ds_read_b128 v[176:179], v191 offset:8320\n" \
  "s_mov_b64 exec, -1\n" \
  "v_exp_f32 v18, v18\n" \
  "v_exp_f32 v19, v19\n" \
  "v_exp_f32 v20, v20\n" \
  "v_exp_f32 v21, v21\n" \
  "v_mfma_f32_32x32x16_bf16 v[48:63], v[164:167], v[140:143], 0\n" \
  "v_exp_f32 v22, v22\n" \
  "v_exp_f32 v23, v23\n" \
  "v_exp_f32 v24, v24\n" \
  "v_exp_f32 v25, v25\n" \
  "v_smfmac_f32_32x32x32_bf16 v[80:95], v[180:183], v[16:23], v188\n" \
  "v_exp_f32 v26, v26\n" \
  "v_exp_f32 v27, v27\n" \
  "v_exp_f32 v28, v28\n" \
  "v_exp_f32 v29, v29\n" \
  "v_mfma_f32_32x32x8_bf16 v[48:63], v[168:169], v[158:159], v[48:63]\n" \
  "v_exp_f32 v30, v30\n" \
  "v_exp_f32 v31, v31\n" \
  "v_exp_f32 v32, v32\n" \
  "v_exp_f32 v33, v33\n" \
  "v_smfmac_f32_32x32x32_bf16 v[80:95], v[184:187], v[24:31], v188\n" \
  "v_exp_f32 v34, v34\n" \
  "v_exp_f32 v35, v35\n" \
  "v_exp_f32 v36, v36\n" \
  "v_exp_f32 v37, v37\n" \
  "s_waitcnt lgkmcnt(0)\n" \
  "v_mfma_f32_32x32x16_bf16 v[0:15], v[160:163], v[128:131], 0\n" \
  "v_exp_f32 v38, v38\n" \
  "v_exp_f32 v39, v39\n" \
  "v_exp_f32 v40, v40\n" \
  "v_exp_f32 v41, v41\n" \
  "v_smfmac_f32_32x32x32_bf16 v[96:111], v[180:183], v[32:39], v188\n" \
  "v_exp_f32 v42, v42\n" \
  "v_exp_f32 v43, v43\n" \
  "v_exp_f32 v44, v44\n" \
  "v_exp_f32 v45, v45\n" \
  "v_mfma_f32_32x32x8_bf16 v[0:15], v[170:171], v[144:145], v[0:15]\n" \
  "v_exp_f32 v46, v46\n" \
  "v_exp_f32 v47, v47\n" \
  "v_exp_f32 v48, v48\n" \
  "v_exp_f32 v49, v49\n" \
  "v_smfmac_f32_32x32x32_bf16 v[96:111], v[184:187], v[40:47], v188\n" \
  "v_exp_f32 v50, v50\n" \
  "v_exp_f32 v51, v51\n" \
  "v_exp_f32 v52, v52\n" \
  "v_exp_f32 v53, v53\n" \
  "v_mfma_f32_32x32x16_bf16 v[16:31], v[160:163], v[132:135], 0\n" \
  "v_exp_f32 v54, v54\n" \
  "v_exp_f32 v55, v55\n" \
  "v_exp_f32 v56, v56\n" \
  "v_exp_f32 v57, v57\n" \
  "v_smfmac_f32_32x32x32_bf16 v[112:127], v[180:183], v[48:55], v188\n" \
  "v_exp_f32 v58, v58\n" \
  "v_exp_f32 v59, v59\n" \
  "v_exp_f32 v60, v60\n" \
  "v_exp_f32 v61, v61\n" \
  "v_mfma_f32_32x32x8_bf16 v[16:31], v[170:171], v[146:147], v[16:31]\n" \
  "v_exp_f32 v62, v62\n" \
  "v_exp_f32 v63, v63\n" \
  "v_exp_f32 v0, v0\n" \
  "v_exp_f32 v1, v1\n" \
  "v_smfmac_f32_32x32x32_bf16 v[112:127], v[184:187], v[56:63], v188\n" \
  "ds_read_b128 v[164:167], v189 offset:3072\n" \
  "v_exp_f32 v2, v2\n" \
  "v_exp_f32 v3, v3\n" \
  "v_exp_f32 v4, v4\n" \
  "v_exp_f32 v5, v5\n" \
  "v_mfma_f32_32x32x16_bf16 v[32:47], v[160:163], v[136:139], 0\n" \
  "v_exp_f32 v6, v6\n" \
  "v_exp_f32 v7, v7\n" \
  "v_exp_f32 v8, v8\n" \
  "v_exp_f32 v9, v9\n" \
  "v_smfmac_f32_32x32x32_bf16 v[64:79], v[172:175], v[0:7], v188\n" \
  "v_exp_f32 v10, v10\n" \
  "v_exp_f32 v11, v11\n" \
  "v_exp_f32 v12, v12\n" \
  "v_exp_f32 v13, v13\n" \
  "v_mfma_f32_32x32x8_bf16 v[32:47], v[170:171], v[148:149], v[32:47]\n" \
  "v_exp_f32 v14, v14\n" \
  "v_exp_f32 v15, v15\n" \
  "v_exp_f32 v16, v16\n" \
  "v_exp_f32 v17, v17\n" \
  "v_smfmac_f32_32x32x32_bf16 v[64:79], v[176:179], v[8:15], v188\n" \
  "s_mov_b64 exec, s[48:49]\n" \
  "ds_read_b128 v[180:183], v191 offset:8960\n" \
  "ds_read_b128 v[184:187], v191 offset:9600\n" \
  "s_mov_b64 exec, -1\n" \
  "v_exp_f32 v18, v18\n" \
  "v_exp_f32 v19, v19\n" \
  "v_exp_f32 v20, v20\n" \
  "v_exp_f32 v21, v21\n" \
  "v_mfma_f32_32x32x16_bf16 v[48:63], v[160:163], v[140:143], 0\n" \
  "v_exp_f32 v22, v22\n" \
  "v_exp_f32 v23, v23\n" \
  "v_exp_f32 v24, v24\n" \
  "v_exp_f32 v25, v25\n" \
  "v_smfmac_f32_32x32x32_bf16 v[80:95], v[172:175], v[16:23], v188\n" \
  "v_exp_f32 v26, v26\n" \
  "v_exp_f32 v27, v27\n" \
  "v_exp_f32 v28, v28\n" \
  "v_exp_f32 v29, v29\n" \
  "v_mfma_f32_32x32x8_bf16 v[48:63], v[170:171], v[150:151], v[48:63]\n" \
  "v_exp_f32 v30, v30\n" \
  "v_exp_f32 v31, v31\n" \
  "v_exp_f32 v32, v32\n" \
  "v_exp_f32 v33, v33\n" \
  "v_smfmac_f32_32x32x32_bf16 v[80:95], v[176:179], v[24:31], v188\n" \
  "v_exp_f32 v34, v34\n" \
  "v_exp_f32 v35, v35\n" \
  "v_exp_f32 v36, v36\n" \
  "v_exp_f32 v37, v37\n" \
  "s_waitcnt lgkmcnt(0)\n" \
  "v_mfma_f32_32x32x16_bf16 v[0:15], v[164:167], v[128:131], 0\n" \
  "v_exp_f32 v38, v38\n" \
  "v_exp_f32 v39, v39\n" \
  "v_exp_f32 v40, v40\n" \
  "v_exp_f32 v41, v41\n" \
  "v_smfmac_f32_32x32x32_bf16 v[96:111], v[172:175], v[32:39], v188\n" \
  "v_exp_f32 v42, v42\n" \
  "v_exp_f32 v43, v43\n" \
  "v_exp_f32 v44, v44\n" \
  "v_exp_f32 v45, v45\n" \
  "v_mfma_f32_32x32x8_bf16 v[0:15], v[170:171], v[152:153], v[0:15]\n" \
  "v_exp_f32 v46, v46\n" \
  "v_exp_f32 v47, v47\n" \
  "v_exp_f32 v48, v48\n" \
  "v_exp_f32 v49, v49\n" \
  "v_smfmac_f32_32x32x32_bf16 v[96:111], v[176:179], v[40:47], v188\n" \
  "v_exp_f32 v50, v50\n" \
  "v_exp_f32 v51, v51\n" \
  "v_exp_f32 v52, v52\n" \
  "v_exp_f32 v53, v53\n" \
  "v_mfma_f32_32x32x16_bf16 v[16:31], v[164:167], v[132:135], 0\n" \
  "v_exp_f32 v54, v54\n" \
  "v_exp_f32 v55, v55\n" \
  "v_exp_f32 v56, v56\n" \
  "v_exp_f32 v57, v57\n" \
  "v_smfmac_f32_32x32x32_bf16 v[112:127], v[172:175], v[48:55], v188\n" \
  "v_exp_f32 v58, v58\n" \
  "v_exp_f32 v59, v59\n" \
  "v_exp_f32 v60, v60\n" \
  "v_exp_f32 v61, v61\n" \
  "v_mfma_f32_32x32x8_bf16 v[16:31], v[170:171], v[154:155], v[16:31]\n" \
  "v_exp_f32 v62, v62\n" \
  "v_exp_f32 v63, v63\n" \
  "v_exp_f32 v0, v0\n" \
  "v_exp_f32 v1, v1\n" \
  "v_smfmac_f32_32x32x32_bf16 v[112:127], v[176:179], v[56:63], v188\n" \
  "v_exp_f32 v2, v2\n" \
  "v_exp_f32 v3, v3\n" \
  "v_exp_f32 v4, v4\n" \
  "v_exp_f32 v5, v5\n" \
  "v_mfma_f32_32x32x16_bf16 v[32:47], v[164:167], v[136:139], 0\n" \
  "v_exp_f32 v6, v6\n" \
  "v_exp_f32 v7, v7\n" \
  "v_exp_f32 v8, v8\n" \
  "v_exp_f32 v9, v9\n" \
  "v_smfmac_f32_32x32x32_bf16 v[64:79], v[180:183], v[0:7], v188\n" \
  "v_exp_f32 v10, v10\n" \
  "v_exp_f32 v11, v11\n" \
  "v_exp_f32 v12, v12\n" \
  "v_exp_f32 v13, v13\n" \
  "v_mfma_f32_32x32x8_bf16 v[32:47], v[170:171], v[156:157], v[32:47]\n" \
  "v_exp_f32 v14, v14\n" \
  "v_exp_f32 v15, v15\n" \
  "v_exp_f32 v16, v16\n" \
  "v_exp_f32 v17, v17\n" \
  "v_smfmac_f32_32x32x32_bf16 v[64:79], v[184:187], v[8:15], v188\n" \
  "v_exp_f32 v18, v18\n" \
  "v_exp_f32 v19, v19\n" \
  "v_exp_f32 v20, v20\n" \
  "v_exp_f32 v21, v21\n" \
  "v_mfma_f32_32x32x16_bf16 v[48:63], v[164:167], v[140:143], 0\n" \
  "v_exp_f32 v22, v22\n" \
  "v_exp_f32 v23, v23\n" \
  "v_exp_f32 v24, v24\n" \
  "v_exp_f32 v25, v25\n" \
  "v_smfmac_f32_32x32x32_bf16 v[80:95], v[180:183], v[16:23], v188\n" \
  "v_exp_f32 v26, v26\n" \
  "v_exp_f32 v27, v27\n" \
  "v_exp_f32 v28, v28\n" \
  "v_exp_f32 v29, v29\n" \
  "v_mfma_f32_32x32x8_bf16 v[48:63], v[170:171], v[158:159], v[48:63]\n" \
  "v_exp_f32 v30, v30\n" \
  "v_exp_f32 v31, v31\n" \
  "v_exp_f32 v32, v32\n" \
  "v_exp_f32 v33, v33\n" \
  "v_smfmac_f32_32x32x32_bf16 v[80:95], v[184:187], v[24:31], v188\n" \
  "v_exp_f32 v34, v34\n" \
  "v_exp_f32 v35, v35\n" \
  "v_exp_f32 v36, v36\n" \
  "v_exp_f32 v37, v37\n" \
  "s_waitcnt lgkmcnt(0)\n" \
  "v_exp_f32 v38, v38\n" \
  "v_exp_f32 v39, v39\n" \
  "v_exp_f32 v40, v40\n" \
  "v_exp_f32 v41, v41\n" \
  "v_smfmac_f32_32x32x32_bf16 v[96:111], v[180:183], v[32:39], v188\n" \
  "v_exp_f32 v42, v42\n" \
  "v_exp_f32 v43, v43\n" \
  "v_exp_f32 v44, v44\n" \
  "v_exp_f32 v45, v45\n" \
  "v_exp_f32 v46, v46\n" \
  "v_exp_f32 v47, v47\n" \
  "v_exp_f32 v48, v48\n" \
  "v_exp_f32 v49, v49\n" \
  "v_smfmac_f32_32x32x32_bf16 v[96:111], v[184:187], v[40:47], v188\n" \
  "v_exp_f32 v50, v50\n" \
  "v_exp_f32 v51, v51\n" \
  "v_exp_f32 v52, v52\n" \
  "v_exp_f32 v53, v53\n" \
  "v_exp_f32 v54, v54\n" \
  "v_exp_f32 v55, v55\n" \
  "v_exp_f32 v56, v56\n" \
  "v_exp_f32 v57, v57\n" \
  "v_smfmac_f32_32x32x32_bf16 v[112:127], v[180:183], v[48:55], v188\n" \
  "v_exp_f32 v58, v58\n" \
  "v_exp_f32 v59, v59\n" \
  "v_exp_f32 v60, v60\n" \
  "v_exp_f32 v61, v61\n" \
  "v_exp_f32 v62, v62\n" \
  "v_exp_f32 v63, v63\n" \
  "s_nop 1\n" \
  "v_smfmac_f32_32x32x32_bf16 v[112:127], v[184:187], v[56:63], v188\n" \
  "s_nop 15\n" \
  ""
#define ATTN_CLOBBERS "v0", "v1", "v2", "v3", "v4", "v5", "v6", "v7", "v8", "v9", "v10", "v11", "v12", "v13", "v14", "v15", "v16", "v17", "v18", "v19", "v20", "v21", "v22", "v23", "v24", "v25", "v26", "v27", "v28", "v29", "v30", "v31", "v32", "v33", "v34", "v35", "v36", "v37", "v38", "v39", "v40", "v41", "v42", "v43", "v44", "v45", "v46", "v47", "v48", "v49", "v50", "v51", "v52", "v53", "v54", "v55", "v56", "v57", "v58", "v59", "v60", "v61", "v62", "v63", "v128", "v129", "v130", "v131", "v132", "v133", "v134", "v135", "v136", "v137", "v138", "v139", "v140", "v141", "v142", "v143", "v144", "v145", "v146", "v147", "v148", "v149", "v150", "v151", "v152", "v153", "v154", "v155", "v156", "v157", "v158", "v159", "v160", "v161", "v162", "v163", "v164", "v165", "v166", "v167", "v168", "v169", "v170", "v171", "v172", "v173", "v174", "v175", "v176", "v177", "v178", "v179", "v180", "v181", "v182", "v183", "v184", "v185", "v186", "v187", "v188", "v189", "v190", "v191", "v192", "v193", "s40", "s41", "s42", "s43", "s44", "s45", "s46", "s47", "s48", "s49", "s50", "s51", "m0", "scc", "vcc", "memory"


__device__ __forceinline__ unsigned pack_bf16(float a, float b) {
  f32x2 v = {a, b};
  bf16x2 r = __builtin_convertvector(v, bf16x2);
  return __builtin_bit_cast(unsigned, r);
}
__device__ __forceinline__ float bf2f(unsigned short x) { return __uint_as_float((unsigned)x << 16); }
__device__ __forceinline__ int swap23(int x) { return (x & ~12) | ((x & 4) << 1) | ((x & 8) >> 1); }

__global__ void qkv_naive(const unsigned short* __restrict__ xA, const unsigned short* __restrict__ wA,
                          const unsigned short* __restrict__ xB, const unsigned short* __restrict__ wB,
                          char* __restrict__ ws) {
  int f = blockIdx.x * 128 + threadIdx.x;
  int token = blockIdx.y, b = blockIdx.z;
  const unsigned short* x = (b ? xB : xA) + token * DM;
  const unsigned short* w = (b ? wB : wA) + f;
  float acc = 0.f;
  for (int k = 0; k < DM; k++) acc += bf2f(x[k]) * bf2f(w[k * 1920]);
  int part = f / DM, ff = f % DM, head = ff / 20, d = ff % 20;
  int m = (head & 1) ? (d + 16) % 20 : d;
  int T = token >> 5, tau = token & 31;
  if (part == 0) acc *= QSCALE;
  unsigned short v = (unsigned short)(pack_bf16(acc, 0.f) & 0xffff);
  char* p;
  if (part == 0) {
    p = ws + WS_Q + (size_t)(b * 32 + head) * QIMG_HEAD;
    p += (m < 16) ? (T * 1024 + (m >> 3) * 512 + tau * 16 + (m & 7) * 2) : (65536 + T * 256 + tau * 8 + (m - 16) * 2);
  } else {
    p = ws + WS_KV + (size_t)(b * 32 + head) * KVIMG_HEAD + (T >> 2) * 10240;
    int t = T & 3, rho = swap23(tau);
    if (part == 1) p += (m < 16) ? (t * 1024 + (m >> 3) * 512 + rho * 16 + (m & 7) * 2) : (4096 + t * 256 + rho * 8 + (m - 16) * 2);
    else p += 5120 + t * 1280 + (rho >> 4) * 640 + ((rho >> 3) & 1) * 320 + m * 16 + (rho & 7) * 2;
  }
  *(unsigned short*)p = v;
}

__global__ void out_naive(const char* __restrict__ ws, const unsigned short* __restrict__ woA,
                          const unsigned short* __restrict__ woB, unsigned short* __restrict__ out) {
  int n = blockIdx.x * 128 + threadIdx.x, token = blockIdx.y, b = blockIdx.z;
  const char* o = ws + WS_O + (size_t)b * OIMG_B + token * 16;
  const unsigned short* w = (b ? woB : woA) + n;
  float acc = 0.f;
  for (int k = 0; k < DM; k++) acc += bf2f(*(const unsigned short*)(o + (k >> 3) * 32768 + (k & 7) * 2)) * bf2f(w[k * DM]);
  out[token * 1280 + b * DM + n] = (unsigned short)(pack_bf16(acc, 0.f) & 0xffff);
}

__device__ __forceinline__ f32x16 qk_tile(const char* kt_main, const char* kt_tail, int lane, u32x4 qm, u32x2 qt) {
  u32x4 km = *(const u32x4*)(kt_main + lane * 16);
  u32x2 kt = *(const u32x2*)(kt_tail + (lane & 31) * 8);
  f32x16 s = {};
  s = __builtin_amdgcn_mfma_f32_32x32x16_bf16(__builtin_bit_cast(bf16x8, km), __builtin_bit_cast(bf16x8, qm), s, 0, 0, 0);
  s = __builtin_amdgcn_mfma_f32_32x32x8bf16_1k(__builtin_bit_cast(s16x4, kt), __builtin_bit_cast(s16x4, qt), s, 0, 0, 0);
  return s;
}

__device__ __forceinline__ f32x16 attn_tile_safe(const char* qb, const char* kv, int qtile, int lane) {
  int h = lane >> 5, r = lane & 31;
  u32x4 qm = *(const u32x4*)(qb + qtile * 1024 + lane * 16);
  u32x2 qt = *(const u32x2*)(qb + 65536 + qtile * 256 + r * 8);
  if (h) { qt[0] = 0; qt[1] = 0; }
  float m = -__builtin_inff();
#pragma unroll 1
  for (int T = 0; T < 64; T++) {
    const char* sl = kv + (T >> 2) * 10240;
    f32x16 s = qk_tile(sl + (T & 3) * 1024, sl + 4096 + (T & 3) * 256, lane, qm, qt);
#pragma unroll
    for (int i = 0; i < 16; i++) m = fmaxf(m, s[i]);
  }
  m = fmaxf(m, __shfl_xor(m, 32));
  f32x16 o = {};
#pragma unroll 1
  for (int T = 0; T < 64; T++) {
    const char* sl = kv + (T >> 2) * 10240;
    f32x16 s = qk_tile(sl + (T & 3) * 1024, sl + 4096 + (T & 3) * 256, lane, qm, qt);
    unsigned p[8];
#pragma unroll
    for (int i = 0; i < 8; i++) p[i] = pack_bf16(__builtin_amdgcn_exp2f(s[2 * i] - m), __builtin_amdgcn_exp2f(s[2 * i + 1] - m));
#pragma unroll
    for (int c = 0; c < 2; c++) {
      const char* vb = sl + 5120 + (T & 3) * 1280 + c * 640 + (r < 20 ? r : 0) * 16 + h * 8;
      u32x2 lo = *(const u32x2*)(vb);
      u32x2 hi = *(const u32x2*)(vb + 320);
      u32x4 va = {lo[0], lo[1], hi[0], hi[1]};
      if (r >= 20) va = u32x4{0x3f803f80u, 0x3f803f80u, 0x3f803f80u, 0x3f803f80u};
      u32x4 pb = {p[4 * c], p[4 * c + 1], p[4 * c + 2], p[4 * c + 3]};
      o = __builtin_amdgcn_mfma_f32_32x32x16_bf16(__builtin_bit_cast(bf16x8, va), __builtin_bit_cast(bf16x8, pb), o, 0, 0, 0);
    }
  }
  return o;
}

__device__ __forceinline__ bool attn_finish(const f32x16& o, char* ob, int head, int token, int h, bool check) {
  float l = o[12];
  float inv = __builtin_amdgcn_rcpf(l);
  float r[12];
  float sum = 0.f;
#pragma unroll
  for (int i = 0; i < 12; i++) { r[i] = o[i] * inv; sum += fabsf(r[i]); }
  if (check) {
    bool ok = (l >= 0x1p-64f) && (l <= 0x1p64f) && (sum <= 3.0e38f);
    if (__builtin_amdgcn_ballot_w64(!ok) != 0) return false;
  }
  int j = head >> 1, odd = head & 1;
  char* p = ob + token * 16;
  u32x2 v0 = {pack_bf16(r[0], r[1]), pack_bf16(r[2], r[3])};
  u32x2 v1 = {pack_bf16(r[4], r[5]), pack_bf16(r[6], r[7])};
  u32x2 v2 = {pack_bf16(r[8], r[9]), pack_bf16(r[10], r[11])};
  *(u32x2*)(p + (5 * j + 3 * odd) * 32768 + h * 8) = v0;
  *(u32x2*)(p + (5 * j + 3 * odd + 1) * 32768 + h * 8) = v1;
  if (h == 0) *(u32x2*)(p + (5 * j + 2) * 32768 + odd * 8) = v2;
  return true;
}

__global__ __launch_bounds__(512, 2) void attn_kernel(char* __restrict__ ws) {
  __shared__ __attribute__((aligned(16))) char smem[163840];
  const int blk = blockIdx.x;
  const int xcd = blk & 7, idx = blk >> 3;
  const int pair = xcd * 4 + (idx >> 3);
  const int b = pair >> 4;
  const int head = 2 * (pair & 15) + (idx & 1), quarter = (idx >> 1) & 3;
  const int w = __builtin_amdgcn_readfirstlane(threadIdx.x >> 6);
  const int qg = w & 3, hf = w >> 2;
  const char* kv = ws + WS_KV + (size_t)(b * 32 + head) * KVIMG_HEAD;
  const char* qb = ws + WS_Q + (size_t)(b * 32 + head) * QIMG_HEAD;
  const int tile0 = quarter * 16 + qg * 4;
  const unsigned ldsbase = (unsigned)(size_t)(__attribute__((address_space(3))) char*)smem;
  f32x16 o0, o1, o2, o3;
  asm volatile(ATTN_ASM
               : "={v[64:79]}"(o0), "={v[80:95]}"(o1), "={v[96:111]}"(o2), "={v[112:127]}"(o3)
               : [img] "s"(kv + hf * 81920), [q] "s"(qb + tile0 * 1024), [qt] "s"(qb + 65536 + tile0 * 256),
                 [lds] "s"(ldsbase + hf * 81920), [qg] "s"(qg)
               : ATTN_CLOBBERS);
  const int lane = threadIdx.x & 63, h = lane >> 5;
  __syncthreads();
  f32x16 ma, mb, xa, xb;
  if (hf == 0) { ma = o0; mb = o1; xa = o2; xb = o3; } else { ma = o2; mb = o3; xa = o0; xb = o1; }
  f32x4* ex = (f32x4*)(smem + w * 8192) + lane;
#pragma unroll
  for (int i = 0; i < 4; i++) {
    ex[i * 64] = f32x4{xa[4 * i], xa[4 * i + 1], xa[4 * i + 2], xa[4 * i + 3]};
    ex[(4 + i) * 64] = f32x4{xb[4 * i], xb[4 * i + 1], xb[4 * i + 2], xb[4 * i + 3]};
  }
  __syncthreads();
  const f32x4* rx = (const f32x4*)(smem + (w ^ 4) * 8192) + lane;
#pragma unroll
  for (int i = 0; i < 4; i++) {
    f32x4 a = rx[i * 64], c = rx[(4 + i) * 64];
#pragma unroll
    for (int k = 0; k < 4; k++) { ma[4 * i + k] += a[k]; mb[4 * i + k] += c[k]; }
  }
  char* ob = ws + WS_O + (size_t)b * OIMG_B;
  const int ta = tile0 + 2 * hf;
  bool oka = attn_finish(ma, ob, head, ta * 32 + (lane & 31), h, true);
  bool okb = attn_finish(mb, ob, head, (ta + 1) * 32 + (lane & 31), h, true);
  if (!(oka && okb)) {
#pragma unroll 1
    for (int i = 0; i < 2; i++) {
      if (i == 0 ? oka : okb) continue;
      f32x16 o = attn_tile_safe(qb, kv, ta + i, lane);
      attn_finish(o, ob, head, (ta + i) * 32 + (lane & 31), h, false);
    }
  }
}

extern "C" void kernel_launch(void* const* d_in, const int* in_sizes, int n_in, void* d_out, int out_size,
                              void* d_ws, size_t ws_size, hipStream_t stream) {
  const unsigned short* xA = (const unsigned short*)d_in[0];
  const unsigned short* wA = (const unsigned short*)d_in[1];
  const unsigned short* woA = (const unsigned short*)d_in[2];
  const unsigned short* xB = (const unsigned short*)d_in[3];
  const unsigned short* wB = (const unsigned short*)d_in[4];
  const unsigned short* woB = (const unsigned short*)d_in[5];
  char* ws = (char*)d_ws;
  qkv_naive<<<dim3(15, SEQ, 2), 128, 0, stream>>>(xA, wA, xB, wB, ws);
  attn_kernel<<<256, 512, 0, stream>>>(ws);
  out_naive<<<dim3(5, SEQ, 2), 128, 0, stream>>>(ws, woA, woB, (unsigned short*)d_out);
}
```

```cpp
#include <hip/hip_runtime.h>
#include <hip/hip_bf16.h>
#include <stdint.h>

typedef float f32x16 __attribute__((ext_vector_type(16)));
typedef float f32x8 __attribute__((ext_vector_type(8)));
typedef float f32x4 __attribute__((ext_vector_type(4)));
typedef float f32x2 __attribute__((ext_vector_type(2)));
typedef __bf16 bf16x2 __attribute__((ext_vector_type(2)));
typedef __bf16 bf16x8 __attribute__((ext_vector_type(8)));
typedef short s16x4 __attribute__((ext_vector_type(4)));
typedef unsigned u32x2 __attribute__((ext_vector_type(2)));
typedef unsigned u32x4 __attribute__((ext_vector_type(4)));

#define DM 640
#define QIMG_HEAD 81920
#define KVIMG_HEAD 163840
#define OIMG_B (80 * 32768)
#define WS_Q 0
#define WS_KV (64 * QIMG_HEAD)
#define WS_O (WS_KV + 64 * KVIMG_HEAD)
#define WS_CNT (WS_O + 2 * OIMG_B)
#define CREP 32
#define CSTR 128
#define WS_QC (WS_CNT + 8 * CREP * CSTR)
#define WS_RS (WS_QC + 480 * 128)
#define LEADER 255
#define SPIN_MAX 0x400000u
#define POLLW 7
#define QSCALE 0.32259442284f

#define ATTN_ASM_A \
  "s_mov_b32 s48, 0xfffff\n" \
  "s_mov_b32 s49, 0xfffff\n" \
  "s_mov_b32 s50, 0\n" \
  "s_mov_b32 s51, -1\n" \
  "v_mbcnt_lo_u32_b32 v193, -1, 0\n" \
  "v_mbcnt_hi_u32_b32 v193, -1, v193\n" \
  "v_lshlrev_b32 v192, 4, v193\n" \
  "v_and_b32 v190, 31, v193\n" \
  "v_lshlrev_b32 v4, 3, v193\n" \
  "s_waitcnt vmcnt(0)\n" \
  "s_barrier\n" \
  "s_cmp_lg_u32 %[role], 2\n" \
  "s_cbranch_scc1 L_synoadd_%=\n" \
  "v_mov_b32 v5, 1\n" \
  "s_mov_b64 exec, 0xff\n" \
  "global_atomic_add %[sy], v5, %[wsb]\n" \
  "s_mov_b64 exec, -1\n" \
  "s_setprio 3\n" \
  "L_synoadd_%=:\n" \
  "v_add_u32 v189, %[lds], v192\n" \
  "v_lshrrev_b32 v191, 5, v193\n" \
  "v_mul_u32_u24 v191, 0x140, v191\n" \
  "v_lshl_add_u32 v191, v190, 4, v191\n" \
  "v_add_u32 v191, %[lds], v191\n" \
  "v_lshlrev_b32 v190, 3, v193\n" \
  "v_add_u32 v190, %[lds], v190\n" \
  "v_mov_b32 v188, 0xdddddddd\n" \
  "v_mov_b32 v204, 0\n" \
  "v_mov_b32 v205, 0\n" \
  "v_mov_b32 v206, 0\n" \
  "v_mov_b32 v207, 0\n" \
  "v_and_b32 v0, 31, v193\n" \
  "v_cmp_eq_u32 vcc, 20, v0\n" \
  "v_mov_b32 v1, 0x3f803f80\n" \
  "s_nop 0\n" \
  "v_cndmask_b32 v1, 0, v1, vcc\n" \
  "v_mov_b32 v172, v1\n" \
  "v_mov_b32 v173, v1\n" \
  "v_mov_b32 v174, v1\n" \
  "v_mov_b32 v175, v1\n" \
  "v_mov_b32 v176, v1\n" \
  "v_mov_b32 v177, v1\n" \
  "v_mov_b32 v178, v1\n" \
  "v_mov_b32 v179, v1\n" \
  "v_mov_b32 v180, v1\n" \
  "v_mov_b32 v181, v1\n" \
  "v_mov_b32 v182, v1\n" \
  "v_mov_b32 v183, v1\n" \
  "v_mov_b32 v184, v1\n" \
  "v_mov_b32 v185, v1\n" \
  "v_mov_b32 v186, v1\n" \
  "v_mov_b32 v187, v1\n" \
  "v_mfma_f32_32x32x16_bf16 v[64:79], v[204:207], v[204:207], 0\n" \
  "v_mfma_f32_32x32x16_bf16 v[80:95], v[204:207], v[204:207], 0\n" \
  "v_mfma_f32_32x32x16_bf16 v[96:111], v[204:207], v[204:207], 0\n" \
  "v_mfma_f32_32x32x16_bf16 v[112:127], v[204:207], v[204:207], 0\n" \
  "v_add_u32 v0, 0, v193\n" \
  "v_mul_u32_u24 v1, 0x199a, v0\n" \
  "v_lshrrev_b32 v1, 16, v1\n" \
  "v_mul_u32_u24 v2, 10, v1\n" \
  "v_sub_u32 v0, v0, v2\n" \
  "v_and_b32 v2, 4, v1\n" \
  "v_and_b32 v3, 8, v1\n" \
  "v_and_b32 v1, -13, v1\n" \
  "v_lshl_or_b32 v1, v2, 1, v1\n" \
  "v_lshrrev_b32 v3, 1, v3\n" \
  "v_or_b32 v1, v1, v3\n" \
  "v_mul_u32_u24 v1, 0x500, v1\n" \
  "v_lshl_add_u32 v195, v0, 4, v1\n" \
  "v_add_u32 v0, 64, v193\n" \
  "v_mul_u32_u24 v1, 0x199a, v0\n" \
  "v_lshrrev_b32 v1, 16, v1\n" \
  "v_mul_u32_u24 v2, 10, v1\n" \
  "v_sub_u32 v0, v0, v2\n" \
  "v_and_b32 v2, 4, v1\n" \
  "v_and_b32 v3, 8, v1\n" \
  "v_and_b32 v1, -13, v1\n" \
  "v_lshl_or_b32 v1, v2, 1, v1\n" \
  "v_lshrrev_b32 v3, 1, v3\n" \
  "v_or_b32 v1, v1, v3\n" \
  "v_mul_u32_u24 v1, 0x500, v1\n" \
  "v_lshl_add_u32 v196, v0, 4, v1\n" \
  "v_add_u32 v0, 128, v193\n" \
  "v_mul_u32_u24 v1, 0x199a, v0\n" \
  "v_lshrrev_b32 v1, 16, v1\n" \
  "v_mul_u32_u24 v2, 10, v1\n" \
  "v_sub_u32 v0, v0, v2\n" \
  "v_and_b32 v2, 4, v1\n" \
  "v_and_b32 v3, 8, v1\n" \
  "v_and_b32 v1, -13, v1\n" \
  "v_lshl_or_b32 v1, v2, 1, v1\n" \
  "v_lshrrev_b32 v3, 1, v3\n" \
  "v_or_b32 v1, v1, v3\n" \
  "v_mul_u32_u24 v1, 0x500, v1\n" \
  "v_lshl_add_u32 v197, v0, 4, v1\n" \
  "v_add_u32 v0, 192, v193\n" \
  "v_mul_u32_u24 v1, 0x199a, v0\n" \
  "v_lshrrev_b32 v1, 16, v1\n" \
  "v_mul_u32_u24 v2, 10, v1\n" \
  "v_sub_u32 v0, v0, v2\n" \
  "v_and_b32 v2, 4, v1\n" \
  "v_and_b32 v3, 8, v1\n" \
  "v_and_b32 v1, -13, v1\n" \
  "v_lshl_or_b32 v1, v2, 1, v1\n" \
  "v_lshrrev_b32 v3, 1, v3\n" \
  "v_or_b32 v1, v1, v3\n" \
  "v_mul_u32_u24 v1, 0x500, v1\n" \
  "v_lshl_add_u32 v198, v0, 4, v1\n" \
  "v_add_u32 v0, 256, v193\n" \
  "v_mul_u32_u24 v1, 0x199a, v0\n" \
  "v_lshrrev_b32 v1, 16, v1\n" \
  "v_mul_u32_u24 v2, 10, v1\n" \
  "v_sub_u32 v0, v0, v2\n" \
  "v_and_b32 v2, 4, v1\n" \
  "v_and_b32 v3, 8, v1\n" \
  "v_and_b32 v1, -13, v1\n" \
  "v_lshl_or_b32 v1, v2, 1, v1\n" \
  "v_lshrrev_b32 v3, 1, v3\n" \
  "v_or_b32 v1, v1, v3\n" \
  "v_mul_u32_u24 v1, 0x500, v1\n" \
  "v_lshl_add_u32 v199, v0, 4, v1\n" \
  "s_mul_i32 s42, %[hf], 81920\n" \
  "s_sub_u32 s42, %[lds], s42\n" \
  "s_mul_i32 s43, %[qg], 12288\n" \
  "s_add_u32 s42, s42, s43\n" \
  "s_add_u32 s42, s42, 51200\n" \
  "s_cmp_eq_u32 %[qg], 3\n" \
  "s_cselect_b32 s43, 51200, 0\n" \
  "s_add_u32 s43, s43, s42\n" \
  "v_add_u32 v200, s43, v192\n" \
  "s_cmp_ge_u32 %[qg], 2\n" \
  "s_cselect_b32 s43, 51200, 0\n" \
  "s_add_u32 s43, s43, s42\n" \
  "s_add_u32 s43, s43, 6144\n" \
  "v_add_u32 v201, s43, v192\n" \
  "s_mul_i32 s44, %[hf], 5\n" \
  "s_cmp_eq_u32 %[hf], 0\n" \
  "s_cselect_b32 s45, 1, 99\n" \
  "s_cselect_b32 s47, 99, 1\n" \
  "s_cmp_eq_u32 %[role], 0\n" \
  "s_cbranch_scc1 L_syend_%=\n" \
  "s_mov_b32 s40, 0x400000\n" \
  "s_cmp_eq_u32 %[role], 3\n" \
  "s_cbranch_scc1 L_syresp_%=\n" \
  "s_mov_b64 exec, 0x3f00\n" \
  "L_sypoll_%=:\n" \
  "global_load_dword v1, %[sy], %[wsb] sc1\n" \
  "s_sub_u32 s40, s40, 1\n" \
  "s_cbranch_scc1 L_sydone_%=\n" \
  "s_waitcnt vmcnt(0)\n" \
  "v_cmp_lt_u32 vcc, v1, %[tg]\n" \
  "s_cbranch_vccnz L_sypoll_%=\n" \
  "s_branch L_sydone_%=\n" \
  "L_syresp_%=:\n" \
  "v_mov_b32 v0, 1\n" \
  "L_syresp2_%=:\n" \
  "global_store_dword %[sy], v0, %[wsb] sc1\n" \
  "global_load_dword v1, %[sy], %[wsb] offset:4 sc1\n" \
  "s_sub_u32 s40, s40, 1\n" \
  "s_cbranch_scc1 L_sydone_%=\n" \
  "s_waitcnt vmcnt(0)\n" \
  "v_cmp_lt_u32 vcc, v1, %[tg]\n" \
  "s_cbranch_vccnz L_syresp2_%=\n" \
  "L_sydone_%=:\n" \
  "s_waitcnt vmcnt(0)\n" \
  "L_sydone2_%=:\n" \
  "s_mov_b64 exec, -1\n" \
  "s_setprio 0\n" \
  "L_syend_%=:\n" \
  "s_barrier\n" \
  "s_mul_i32 s42, %[qg], 0x1400\n" \
  "s_mov_b64 s[40:41], %[img]\n" \
  "s_add_u32 m0, %[lds], s42\n" \
  "s_add_u32 s40, s40, s42\n" \
  "s_addc_u32 s41, s41, 0\n" \
  "global_load_lds_dwordx4 v192, s[40:41] offset:0 sc1\n" \
  "global_load_lds_dwordx4 v192, s[40:41] offset:1024 sc1\n" \
  "global_load_lds_dwordx4 v192, s[40:41] offset:2048 sc1\n" \
  "global_load_lds_dwordx4 v192, s[40:41] offset:3072 sc1\n" \
  "s_add_u32 m0, m0, 0x1000\n" \
  "s_add_u32 s40, s40, 0x1000\n" \
  "s_addc_u32 s41, s41, 0\n" \
  "global_load_lds_dwordx4 v192, s[40:41] offset:0 sc1\n" \
  "global_load_dwordx4 v[128:131], v192, %[q] offset:0 sc1\n" \
  "global_load_dwordx4 v[132:135], v192, %[q] offset:1024 sc1\n" \
  "global_load_dwordx4 v[136:139], v192, %[q] offset:2048 sc1\n" \
  "global_load_dwordx4 v[140:143], v192, %[q] offset:3072 sc1\n" \
  "global_load_dwordx2 v[144:145], v4, %[qt] offset:0 sc1\n" \
  "global_load_dwordx2 v[146:147], v4, %[qt] offset:512 sc1\n" \
  "s_mov_b64 s[40:41], %[img]\n" \
  "s_mul_i32 s42, %[qg], 0x3c00\n" \
  "s_add_u32 s42, s42, 0x5000\n" \
  "s_add_u32 s40, s40, s42\n" \
  "s_addc_u32 s41, s41, 0\n" \
  "s_add_u32 m0, %[lds], s42\n" \
  "s_mov_b32 s46, 1\n" \
  "s_waitcnt vmcnt(0)\n" \
  "s_barrier\n" \
  "s_getpc_b64 s[42:43]\n" \
  "L_pc_%=:\n" \
  "v_min_u32 v194, L_attn_end-L_pc_%=, %[pfo]\n" \
  "global_load_dword v194, v194, s[42:43]\n" \
  "s_cmp_eq_u32 %[hf], 0\n" \
  "s_cselect_b32 s42, 5, 99\n" \
  "ds_read_b128 v[160:163], v189 offset:0\n" \
  "ds_read_b64 v[168:169], v190 offset:4096\n" \
  "s_mov_b64 exec, s[48:49]\n" \
  "ds_read_b128 v[172:175], v191 offset:5120\n" \
  "ds_read_b128 v[176:179], v191 offset:5760\n" \
  "s_mov_b64 exec, -1\n" \
  "s_waitcnt lgkmcnt(0)\n" \
  "v_mfma_f32_32x32x4_2b_bf16 v[0:31], v[168:169], v[144:145], 0 cbsz:1\n" \
  "s_nop 15\n" \
  "s_nop 1\n" \
  "v_mfma_f32_32x32x16_bf16 v[0:15], v[160:163], v[128:131], v[0:15]\n" \
  "v_mfma_f32_32x32x16_bf16 v[16:31], v[160:163], v[132:135], v[16:31]\n" \
  "s_nop 15\n" \
  "s_setprio 3\n" \
  "v_exp_f32 v0, v0\n" \
  "v_exp_f32 v1, v1\n" \
  "ds_read_b128 v[164:167], v189 offset:1024\n" \
  "v_exp_f32 v2, v2\n" \
  "v_exp_f32 v3, v3\n" \
  "v_exp_f32 v4, v4\n" \
  "v_exp_f32 v5, v5\n" \
  "v_mfma_f32_32x32x4_2b_bf16 v[32:63], v[168:169], v[146:147], 0 cbsz:1\n" \
  "ds_read_b64 v[170:171], v190 offset:4608\n" \
  "v_exp_f32 v6, v6\n" \
  "v_exp_f32 v7, v7\n" \
  "v_exp_f32 v8, v8\n" \
  "v_exp_f32 v9, v9\n" \
  "v_exp_f32 v10, v10\n" \
  "v_exp_f32 v11, v11\n" \
  "v_exp_f32 v12, v12\n" \
  "v_exp_f32 v13, v13\n" \
  "v_smfmac_f32_32x32x32_bf16 v[64:79], v[172:175], v[0:7], v188\n" \
  "global_load_lds_dwordx4 v192, s[40:41] offset:0 sc1\n" \
  "v_exp_f32 v14, v14\n" \
  "v_exp_f32 v15, v15\n" \
  "v_exp_f32 v16, v16\n" \
  "v_exp_f32 v17, v17\n" \
  "v_smfmac_f32_32x32x32_bf16 v[64:79], v[176:179], v[8:15], v188\n" \
  "s_mov_b64 exec, s[48:49]\n" \
  "ds_read_b128 v[180:183], v191 offset:6400\n" \
  "ds_read_b128 v[184:187], v191 offset:7040\n" \
  "s_mov_b64 exec, -1\n" \
  "v_exp_f32 v18, v18\n" \
  "v_exp_f32 v19, v19\n" \
  "v_exp_f32 v20, v20\n" \
  "v_exp_f32 v21, v21\n" \
  "v_mfma_f32_32x32x16_bf16 v[32:47], v[160:163], v[136:139], v[32:47]\n" \
  "v_exp_f32 v22, v22\n" \
  "v_exp_f32 v23, v23\n" \
  "v_exp_f32 v24, v24\n" \
  "v_exp_f32 v25, v25\n" \
  "v_smfmac_f32_32x32x32_bf16 v[80:95], v[172:175], v[16:23], v188\n" \
  "global_load_lds_dwordx4 v192, s[40:41] offset:1024 sc1\n" \
  "v_exp_f32 v26, v26\n" \
  "v_exp_f32 v27, v27\n" \
  "v_exp_f32 v28, v28\n" \
  "v_exp_f32 v29, v29\n" \
  "v_mfma_f32_32x32x16_bf16 v[48:63], v[160:163], v[140:143], v[48:63]\n" \
  "v_exp_f32 v30, v30\n" \
  "v_exp_f32 v31, v31\n" \
  "v_exp_f32 v32, v32\n" \
  "v_exp_f32 v33, v33\n" \
  "v_smfmac_f32_32x32x32_bf16 v[80:95], v[176:179], v[24:31], v188\n" \
  "v_exp_f32 v34, v34\n" \
  "v_exp_f32 v35, v35\n" \
  "v_exp_f32 v36, v36\n" \
  "v_exp_f32 v37, v37\n" \
  "s_waitcnt lgkmcnt(0)\n" \
  "v_mfma_f32_32x32x4_2b_bf16 v[0:31], v[168:169], v[144:145], 0 cbsz:1 abid:1\n" \
  "v_exp_f32 v38, v38\n" \
  "v_exp_f32 v39, v39\n" \
  "v_exp_f32 v40, v40\n" \
  "v_exp_f32 v41, v41\n" \
  "v_exp_f32 v42, v42\n" \
  "v_exp_f32 v43, v43\n" \
  "v_exp_f32 v44, v44\n" \
  "v_exp_f32 v45, v45\n" \
  "v_smfmac_f32_32x32x32_bf16 v[96:111], v[172:175], v[32:39], v188\n" \
  "global_load_lds_dwordx4 v192, s[40:41] offset:2048 sc1\n" \
  "v_exp_f32 v46, v46\n" \
  "v_exp_f32 v47, v47\n" \
  "v_exp_f32 v48, v48\n" \
  "v_exp_f32 v49, v49\n" \
  "v_smfmac_f32_32x32x32_bf16 v[96:111], v[176:179], v[40:47], v188\n" \
  "v_exp_f32 v50, v50\n" \
  "v_exp_f32 v51, v51\n" \
  "v_exp_f32 v52, v52\n" \
  "v_exp_f32 v53, v53\n" \
  "v_mfma_f32_32x32x16_bf16 v[0:15], v[164:167], v[128:131], v[0:15]\n" \
  "v_exp_f32 v54, v54\n" \
  "v_exp_f32 v55, v55\n" \
  "v_exp_f32 v56, v56\n" \
  "v_exp_f32 v57, v57\n" \
  "v_smfmac_f32_32x32x32_bf16 v[112:127], v[172:175], v[48:55], v188\n" \
  "global_load_lds_dwordx4 v192, s[40:41] offset:3072 sc1\n" \
  "v_exp_f32 v58, v58\n" \
  "v_exp_f32 v59, v59\n" \
  "v_exp_f32 v60, v60\n" \
  "v_exp_f32 v61, v61\n" \
  "v_mfma_f32_32x32x16_bf16 v[16:31], v[164:167], v[132:135], v[16:31]\n" \
  "v_exp_f32 v62, v62\n" \
  "v_exp_f32 v63, v63\n" \
  "v_exp_f32 v0, v0\n" \
  "v_exp_f32 v1, v1\n" \
  "v_smfmac_f32_32x32x32_bf16 v[112:127], v[176:179], v[56:63], v188\n" \
  "ds_read_b128 v[160:163], v189 offset:2048\n" \
  "v_exp_f32 v2, v2\n" \
  "v_exp_f32 v3, v3\n" \
  "v_exp_f32 v4, v4\n" \
  "v_exp_f32 v5, v5\n" \
  "v_mfma_f32_32x32x4_2b_bf16 v[32:63], v[168:169], v[146:147], 0 cbsz:1 abid:1\n" \
  "s_add_u32 m0, m0, 0x1000\n" \
  "s_add_u32 s40, s40, 0x1000\n" \
  "s_addc_u32 s41, s41, 0\n" \
  "v_exp_f32 v6, v6\n" \
  "v_exp_f32 v7, v7\n" \
  "v_exp_f32 v8, v8\n" \
  "v_exp_f32 v9, v9\n" \
  "v_exp_f32 v10, v10\n" \
  "v_exp_f32 v11, v11\n" \
  "v_exp_f32 v12, v12\n" \
  "v_exp_f32 v13, v13\n" \
  "v_smfmac_f32_32x32x32_bf16 v[64:79], v[180:183], v[0:7], v188\n" \
  "global_load_lds_dwordx4 v192, s[40:41] offset:0 sc1\n" \
  "v_exp_f32 v14, v14\n" \
  "v_exp_f32 v15, v15\n" \
  "v_exp_f32 v16, v16\n" \
  "v_exp_f32 v17, v17\n" \
  "v_smfmac_f32_32x32x32_bf16 v[64:79], v[184:187], v[8:15], v188\n" \
  "s_mov_b64 exec, s[48:49]\n" \
  "ds_read_b128 v[172:175], v191 offset:7680\n" \
  "ds_read_b128 v[176:179], v191 offset:8320\n" \
  "s_mov_b64 exec, -1\n" \
  "v_exp_f32 v18, v18\n" \
  "v_exp_f32 v19, v19\n" \
  "v_exp_f32 v20, v20\n" \
  "v_exp_f32 v21, v21\n" \
  "v_mfma_f32_32x32x16_bf16 v[32:47], v[164:167], v[136:139], v[32:47]\n" \
  "v_exp_f32 v22, v22\n" \
  "v_exp_f32 v23, v23\n" \
  "v_exp_f32 v24, v24\n" \
  "v_exp_f32 v25, v25\n" \
  "v_smfmac_f32_32x32x32_bf16 v[80:95], v[180:183], v[16:23], v188\n" \
  "global_load_lds_dwordx4 v192, s[40:41] offset:1024 sc1\n" \
  "v_exp_f32 v26, v26\n" \
  "v_exp_f32 v27, v27\n" \
  "v_exp_f32 v28, v28\n" \
  "v_exp_f32 v29, v29\n" \
  "v_mfma_f32_32x32x16_bf16 v[48:63], v[164:167], v[140:143], v[48:63]\n" \
  "v_exp_f32 v30, v30\n" \
  "v_exp_f32 v31, v31\n" \
  "v_exp_f32 v32, v32\n" \
  "v_exp_f32 v33, v33\n" \
  "v_smfmac_f32_32x32x32_bf16 v[80:95], v[184:187], v[24:31], v188\n" \
  "v_exp_f32 v34, v34\n" \
  "v_exp_f32 v35, v35\n" \
  "v_exp_f32 v36, v36\n" \
  "v_exp_f32 v37, v37\n" \
  "s_waitcnt lgkmcnt(0)\n" \
  "v_mfma_f32_32x32x4_2b_bf16 v[0:31], v[170:171], v[144:145], 0 cbsz:1\n" \
  "v_exp_f32 v38, v38\n" \
  "v_exp_f32 v39, v39\n" \
  "v_exp_f32 v40, v40\n" \
  "v_exp_f32 v41, v41\n" \
  "v_exp_f32 v42, v42\n" \
  "v_exp_f32 v43, v43\n" \
  "v_exp_f32 v44, v44\n" \
  "v_exp_f32 v45, v45\n" \
  "v_smfmac_f32_32x32x32_bf16 v[96:111], v[180:183], v[32:39], v188\n" \
  "global_load_lds_dwordx4 v192, s[40:41] offset:2048 sc1\n" \
  "v_exp_f32 v46, v46\n" \
  "v_exp_f32 v47, v47\n" \
  "v_exp_f32 v48, v48\n" \
  "v_exp_f32 v49, v49\n" \
  "v_smfmac_f32_32x32x32_bf16 v[96:111], v[184:187], v[40:47], v188\n" \
  "v_exp_f32 v50, v50\n" \
  "v_exp_f32 v51, v51\n" \
  "v_exp_f32 v52, v52\n" \
  "v_exp_f32 v53, v53\n" \
  "v_mfma_f32_32x32x16_bf16 v[0:15], v[160:163], v[128:131], v[0:15]\n" \
  "v_exp_f32 v54, v54\n" \
  "v_exp_f32 v55, v55\n" \
  "v_exp_f32 v56, v56\n" \
  "v_exp_f32 v57, v57\n" \
  "v_smfmac_f32_32x32x32_bf16 v[112:127], v[180:183], v[48:55], v188\n" \
  "global_load_lds_dwordx4 v192, s[40:41] offset:3072 sc1\n" \
  "v_exp_f32 v58, v58\n" \
  "v_exp_f32 v59, v59\n" \
  "v_exp_f32 v60, v60\n" \
  "v_exp_f32 v61, v61\n" \
  "v_mfma_f32_32x32x16_bf16 v[16:31], v[160:163], v[132:135], v[16:31]\n" \
  "v_exp_f32 v62, v62\n" \
  "v_exp_f32 v63, v63\n" \
  "v_exp_f32 v0, v0\n" \
  "v_exp_f32 v1, v1\n" \
  "v_smfmac_f32_32x32x32_bf16 v[112:127], v[184:187], v[56:63], v188\n" \
  "ds_read_b128 v[164:167], v189 offset:3072\n" \
  "v_exp_f32 v2, v2\n" \
  "v_exp_f32 v3, v3\n" \
  "v_exp_f32 v4, v4\n" \
  "v_exp_f32 v5, v5\n" \
  "v_mfma_f32_32x32x4_2b_bf16 v[32:63], v[170:171], v[146:147], 0 cbsz:1\n" \
  "s_add_u32 m0, m0, 0x1000\n" \
  "s_add_u32 s40, s40, 0x1000\n" \
  "s_addc_u32 s41, s41, 0\n" \
  "v_exp_f32 v6, v6\n" \
  "v_exp_f32 v7, v7\n" \
  "v_exp_f32 v8, v8\n" \
  "v_exp_f32 v9, v9\n" \
  "v_exp_f32 v10, v10\n" \
  "v_exp_f32 v11, v11\n" \
  "v_exp_f32 v12, v12\n" \
  "v_exp_f32 v13, v13\n" \
  "v_smfmac_f32_32x32x32_bf16 v[64:79], v[172:175], v[0:7], v188\n" \
  "global_load_lds_dwordx4 v192, s[40:41] offset:0 sc1\n" \
  "v_exp_f32 v14, v14\n" \
  "v_exp_f32 v15, v15\n" \
  "v_exp_f32 v16, v16\n" \
  "v_exp_f32 v17, v17\n" \
  "v_smfmac_f32_32x32x32_bf16 v[64:79], v[176:179], v[8:15], v188\n" \
  "s_mov_b64 exec, s[48:49]\n" \
  "ds_read_b128 v[180:183], v191 offset:8960\n" \
  "ds_read_b128 v[184:187], v191 offset:9600\n" \
  "s_mov_b64 exec, -1\n" \
  "v_exp_f32 v18, v18\n" \
  "v_exp_f32 v19, v19\n" \
  "v_exp_f32 v20, v20\n" \
  "v_exp_f32 v21, v21\n" \
  "v_mfma_f32_32x32x16_bf16 v[32:47], v[160:163], v[136:139], v[32:47]\n" \
  "v_exp_f32 v22, v22\n" \
  "v_exp_f32 v23, v23\n" \
  "v_exp_f32 v24, v24\n" \
  "v_exp_f32 v25, v25\n" \
  "v_smfmac_f32_32x32x32_bf16 v[80:95], v[172:175], v[16:23], v188\n" \
  "global_load_lds_dwordx4 v192, s[40:41] offset:1024 sc1\n" \
  "v_exp_f32 v26, v26\n" \
  "v_exp_f32 v27, v27\n" \
  "v_exp_f32 v28, v28\n" \
  "v_exp_f32 v29, v29\n" \
  "v_mfma_f32_32x32x16_bf16 v[48:63], v[160:163], v[140:143], v[48:63]\n" \
  "v_exp_f32 v30, v30\n" \
  "v_exp_f32 v31, v31\n" \
  "v_exp_f32 v32, v32\n" \
  "v_exp_f32 v33, v33\n" \
  "v_smfmac_f32_32x32x32_bf16 v[80:95], v[176:179], v[24:31], v188\n" \
  "v_exp_f32 v34, v34\n" \
  "v_exp_f32 v35, v35\n" \
  "v_exp_f32 v36, v36\n" \
  "v_exp_f32 v37, v37\n" \
  "s_waitcnt lgkmcnt(0)\n" \
  "v_mfma_f32_32x32x4_2b_bf16 v[0:31], v[170:171], v[144:145], 0 cbsz:1 abid:1\n" \
  "v_exp_f32 v38, v38\n" \
  "v_exp_f32 v39, v39\n" \
  "v_exp_f32 v40, v40\n" \
  "v_exp_f32 v41, v41\n" \
  "v_exp_f32 v42, v42\n" \
  "v_exp_f32 v43, v43\n" \
  "v_exp_f32 v44, v44\n" \
  "v_exp_f32 v45, v45\n" \
  "v_smfmac_f32_32x32x32_bf16 v[96:111], v[172:175], v[32:39], v188\n" \
  "global_load_lds_dwordx4 v192, s[40:41] offset:2048 sc1\n" \
  "v_exp_f32 v46, v46\n" \
  "v_exp_f32 v47, v47\n" \
  "v_exp_f32 v48, v48\n" \
  "v_exp_f32 v49, v49\n" \
  "v_smfmac_f32_32x32x32_bf16 v[96:111], v[176:179], v[40:47], v188\n" \
  "v_exp_f32 v50, v50\n" \
  "v_exp_f32 v51, v51\n" \
  "v_exp_f32 v52, v52\n" \
  "v_exp_f32 v53, v53\n" \
  "v_mfma_f32_32x32x16_bf16 v[0:15], v[164:167], v[128:131], v[0:15]\n" \
  "v_exp_f32 v54, v54\n" \
  "v_exp_f32 v55, v55\n" \
  "v_exp_f32 v56, v56\n" \
  "v_exp_f32 v57, v57\n" \
  "v_smfmac_f32_32x32x32_bf16 v[112:127], v[172:175], v[48:55], v188\n" \
  "global_load_lds_dwordx4 v192, s[40:41] offset:3072 sc1\n" \
  "v_exp_f32 v58, v58\n" \
  "v_exp_f32 v59, v59\n" \
  "v_exp_f32 v60, v60\n" \
  "v_exp_f32 v61, v61\n" \
  "v_mfma_f32_32x32x16_bf16 v[16:31], v[164:167], v[132:135], v[16:31]\n" \
  "v_exp_f32 v62, v62\n" \
  "v_exp_f32 v63, v63\n" \
  "v_exp_f32 v0, v0\n" \
  "v_exp_f32 v1, v1\n" \
  "v_smfmac_f32_32x32x32_bf16 v[112:127], v[176:179], v[56:63], v188\n" \
  "ds_read_b128 v[160:163], v189 offset:10240\n" \
  "v_exp_f32 v2, v2\n" \
  "v_exp_f32 v3, v3\n" \
  "v_exp_f32 v4, v4\n" \
  "v_exp_f32 v5, v5\n" \
  "v_mfma_f32_32x32x4_2b_bf16 v[32:63], v[170:171], v[146:147], 0 cbsz:1 abid:1\n" \
  "ds_read_b64 v[168:169], v190 offset:14336\n" \
  "s_add_u32 m0, m0, 0x1000\n" \
  "s_add_u32 s40, s40, 0x1000\n" \
  "s_addc_u32 s41, s41, 0\n" \
  "v_exp_f32 v6, v6\n" \
  "v_exp_f32 v7, v7\n" \
  "v_exp_f32 v8, v8\n" \
  "v_exp_f32 v9, v9\n" \
  "v_exp_f32 v10, v10\n" \
  "v_exp_f32 v11, v11\n" \
  "v_exp_f32 v12, v12\n" \
  "v_exp_f32 v13, v13\n" \
  "v_smfmac_f32_32x32x32_bf16 v[64:79], v[180:183], v[0:7], v188\n" \
  "global_load_lds_dwordx4 v192, s[40:41] offset:0 sc1\n" \
  "v_exp_f32 v14, v14\n" \
  "v_exp_f32 v15, v15\n" \
  "v_exp_f32 v16, v16\n" \
  "v_exp_f32 v17, v17\n" \
  "v_smfmac_f32_32x32x32_bf16 v[64:79], v[184:187], v[8:15], v188\n" \
  "s_mov_b64 exec, s[48:49]\n" \
  "ds_read_b128 v[172:175], v191 offset:15360\n" \
  "ds_read_b128 v[176:179], v191 offset:16000\n" \
  "s_mov_b64 exec, -1\n" \
  "v_exp_f32 v18, v18\n" \
  "v_exp_f32 v19, v19\n" \
  "v_exp_f32 v20, v20\n" \
  "v_exp_f32 v21, v21\n" \
  "v_mfma_f32_32x32x16_bf16 v[32:47], v[164:167], v[136:139], v[32:47]\n" \
  "v_exp_f32 v22, v22\n" \
  "v_exp_f32 v23, v23\n" \
  "v_exp_f32 v24, v24\n" \
  "v_exp_f32 v25, v25\n" \
  "v_smfmac_f32_32x32x32_bf16 v[80:95], v[180:183], v[16:23], v188\n" \
  "global_load_lds_dwordx4 v192, s[40:41] offset:1024 sc1\n" \
  "v_exp_f32 v26, v26\n" \
  "v_exp_f32 v27, v27\n" \
  "v_exp_f32 v28, v28\n" \
  "v_exp_f32 v29, v29\n" \
  "v_mfma_f32_32x32x16_bf16 v[48:63], v[164:167], v[140:143], v[48:63]\n" \
  "v_exp_f32 v30, v30\n" \
  "v_exp_f32 v31, v31\n" \
  "v_exp_f32 v32, v32\n" \
  "v_exp_f32 v33, v33\n" \
  "v_smfmac_f32_32x32x32_bf16 v[80:95], v[184:187], v[24:31], v188\n" \
  "v_exp_f32 v34, v34\n" \
  "v_exp_f32 v35, v35\n" \
  "v_exp_f32 v36, v36\n" \
  "v_exp_f32 v37, v37\n" \
  "s_waitcnt lgkmcnt(0)\n" \
  "v_mfma_f32_32x32x4_2b_bf16 v[0:31], v[168:169], v[144:145], 0 cbsz:1\n" \
  "v_exp_f32 v38, v38\n" \
  "v_exp_f32 v39, v39\n" \
  "v_exp_f32 v40, v40\n" \
  "v_exp_f32 v41, v41\n" \
  "v_exp_f32 v42, v42\n" \
  "v_exp_f32 v43, v43\n" \
  "v_exp_f32 v44, v44\n" \
  "v_exp_f32 v45, v45\n" \
  "v_smfmac_f32_32x32x32_bf16 v[96:111], v[180:183], v[32:39], v188\n" \
  "global_load_lds_dwordx4 v192, s[40:41] offset:2048 sc1\n" \
  "v_exp_f32 v46, v46\n" \
  "v_exp_f32 v47, v47\n" \
  "v_exp_f32 v48, v48\n" \
  "v_exp_f32 v49, v49\n" \
  "v_smfmac_f32_32x32x32_bf16 v[96:111], v[184:187], v[40:47], v188\n" \
  "v_exp_f32 v50, v50\n" \
  "v_exp_f32 v51, v51\n" \
  "v_exp_f32 v52, v52\n" \
  "v_exp_f32 v53, v53\n" \
  "v_mfma_f32_32x32x16_bf16 v[0:15], v[160:163], v[128:131], v[0:15]\n" \
  "v_exp_f32 v54, v54\n" \
  "v_exp_f32 v55, v55\n" \
  "v_exp_f32 v56, v56\n" \
  "v_exp_f32 v57, v57\n" \
  "v_smfmac_f32_32x32x32_bf16 v[112:127], v[180:183], v[48:55], v188\n" \
  "v_add_u32 v189, 0x2800, v189\n" \
  "v_add_u32 v190, 0x2800, v190\n" \
  "v_add_u32 v191, 0x2800, v191\n" \
  "v_exp_f32 v58, v58\n" \
  "v_exp_f32 v59, v59\n" \
  "v_exp_f32 v60, v60\n" \
  "v_exp_f32 v61, v61\n" \
  "v_mfma_f32_32x32x16_bf16 v[16:31], v[160:163], v[132:135], v[16:31]\n" \
  "v_exp_f32 v62, v62\n" \
  "v_exp_f32 v63, v63\n" \
  "L_loop_%=:\n" \
  "s_bitcmp1_b32 0x22, s46\n" \
  "s_cbranch_scc0 L_nolev_%=\n" \
  "s_cmp_eq_u32 s46, 1\n" \
  "s_cbranch_scc0 L_lev1_%=\n" \
  "s_setprio 2\n" \
  "s_branch L_nolev_%=\n" \
  "L_lev1_%=:\n" \
  "s_setprio 1\n" \
  "L_nolev_%=:\n" \
  "s_cmp_lg_u32 s46, s44\n" \
  "s_cbranch_scc1 L_nx1_%=\n" \
  "s_barrier\n" \
  "s_mov_b64 s[40:41], %[wo2]\n" \
  "s_mul_i32 s43, %[qg], 0x1400\n" \
  "s_add_u32 s43, s43, %[lds]\n" \
  "s_sub_u32 s43, s43, 81920\n" \
  "s_cmp_ge_u32 %[qg], 2\n" \
  "s_cselect_b32 s50, 30720, 0\n" \
  "s_add_u32 s50, s50, s43\n" \
  "s_add_u32 m0, s43, 0\n" \
  "s_nop 0\n" \
  "global_load_lds_dwordx4 v195, s[40:41]\n" \
  "s_add_u32 m0, s43, 1024\n" \
  "s_nop 0\n" \
  "global_load_lds_dwordx4 v196, s[40:41]\n" \
  "s_add_u32 m0, s43, 2048\n" \
  "s_nop 0\n" \
  "global_load_lds_dwordx4 v197, s[40:41]\n" \
  "s_add_u32 m0, s43, 3072\n" \
  "s_nop 0\n" \
  "global_load_lds_dwordx4 v198, s[40:41]\n" \
  "s_add_u32 m0, s43, 4096\n" \
  "s_nop 0\n" \
  "global_load_lds_dwordx4 v199, s[40:41]\n" \
  "s_add_u32 s40, s40, 163840\n" \
  "s_addc_u32 s41, s41, 0\n" \
  "s_add_u32 m0, s43, 20480\n" \
  "s_nop 0\n" \
  "global_load_lds_dwordx4 v195, s[40:41]\n" \
  "s_add_u32 m0, s43, 21504\n" \
  "s_nop 0\n" \
  "global_load_lds_dwordx4 v196, s[40:41]\n" \
  "s_add_u32 m0, s43, 22528\n" \
  "s_nop 0\n" \
  "global_load_lds_dwordx4 v197, s[40:41]\n" \
  "s_add_u32 m0, s43, 23552\n" \
  "s_nop 0\n" \
  "global_load_lds_dwordx4 v198, s[40:41]\n" \
  "s_add_u32 m0, s43, 24576\n" \
  "s_nop 0\n" \
  "global_load_lds_dwordx4 v199, s[40:41]\n" \
  "s_add_u32 s40, s40, 163840\n" \
  "s_addc_u32 s41, s41, 0\n" \
  "s_add_u32 m0, s50, 40960\n" \
  "s_nop 0\n" \
  "global_load_lds_dwordx4 v195, s[40:41]\n" \
  "s_add_u32 m0, s50, 41984\n" \
  "s_nop 0\n" \
  "global_load_lds_dwordx4 v196, s[40:41]\n" \
  "s_add_u32 m0, s50, 43008\n" \
  "s_nop 0\n" \
  "global_load_lds_dwordx4 v197, s[40:41]\n" \
  "s_add_u32 m0, s50, 44032\n" \
  "s_nop 0\n" \
  "global_load_lds_dwordx4 v198, s[40:41]\n" \
  "s_add_u32 m0, s50, 45056\n" \
  "s_nop 0\n" \
  "global_load_lds_dwordx4 v199, s[40:41]\n" \
  "s_add_u32 s40, s40, 163840\n" \
  "s_addc_u32 s41, s41, 0\n" \
  "s_add_u32 m0, s43, 92160\n" \
  "s_nop 0\n" \
  "global_load_lds_dwordx4 v195, s[40:41]\n" \
  "s_add_u32 m0, s43, 93184\n" \
  "s_nop 0\n" \
  "global_load_lds_dwordx4 v196, s[40:41]\n" \
  "s_add_u32 m0, s43, 94208\n" \
  "s_nop 0\n" \
  "global_load_lds_dwordx4 v197, s[40:41]\n" \
  "s_add_u32 m0, s43, 95232\n" \
  "s_nop 0\n" \
  "global_load_lds_dwordx4 v198, s[40:41]\n" \
  "s_add_u32 m0, s43, 96256\n" \
  "s_nop 0\n" \
  "global_load_lds_dwordx4 v199, s[40:41]\n" \
  "s_add_u32 s40, s40, 163840\n" \
  "s_addc_u32 s41, s41, 0\n" \
  "s_add_u32 m0, s43, 112640\n" \
  "s_nop 0\n" \
  "global_load_lds_dwordx4 v195, s[40:41]\n" \
  "s_add_u32 m0, s43, 113664\n" \
  "s_nop 0\n" \
  "global_load_lds_dwordx4 v196, s[40:41]\n" \
  "s_add_u32 m0, s43, 114688\n" \
  "s_nop 0\n" \
  "global_load_lds_dwordx4 v197, s[40:41]\n" \
  "s_add_u32 m0, s43, 115712\n" \
  "s_nop 0\n" \
  "global_load_lds_dwordx4 v198, s[40:41]\n" \
  "s_add_u32 m0, s43, 116736\n" \
  "s_nop 0\n" \
  "global_load_lds_dwordx4 v199, s[40:41]\n" \
  "L_nx1_%=:\n" \
  "v_exp_f32 v0, v0\n" \
  "v_exp_f32 v1, v1\n" \
  "v_smfmac_f32_32x32x32_bf16 v[112:127], v[184:187], v[56:63], v188\n" \
  "ds_read_b128 v[164:167], v189 offset:1024\n" \
  "v_exp_f32 v2, v2\n" \
  "v_exp_f32 v3, v3\n" \
  "v_exp_f32 v4, v4\n" \
  "v_exp_f32 v5, v5\n" \
  "v_mfma_f32_32x32x4_2b_bf16 v[32:63], v[168:169], v[146:147], 0 cbsz:1\n" \
  "ds_read_b64 v[170:171], v190 offset:4608\n" \
  "v_exp_f32 v6, v6\n" \
  "v_exp_f32 v7, v7\n" \
  "v_exp_f32 v8, v8\n" \
  "v_exp_f32 v9, v9\n" \
  "v_exp_f32 v10, v10\n" \
  "v_exp_f32 v11, v11\n" \
  "v_exp_f32 v12, v12\n" \
  "v_exp_f32 v13, v13\n" \
  "v_smfmac_f32_32x32x32_bf16 v[64:79], v[172:175], v[0:7], v188\n" \
  "v_exp_f32 v14, v14\n" \
  "v_exp_f32 v15, v15\n" \
  "v_exp_f32 v16, v16\n" \
  "v_exp_f32 v17, v17\n" \
  "v_smfmac_f32_32x32x32_bf16 v[64:79], v[176:179], v[8:15], v188\n" \
  "s_mov_b64 exec, s[48:49]\n" \
  "ds_read_b128 v[180:183], v191 offset:6400\n" \
  "ds_read_b128 v[184:187], v191 offset:7040\n" \
  "s_mov_b64 exec, -1\n" \
  "v_exp_f32 v18, v18\n" \
  "v_exp_f32 v19, v19\n" \
  "v_exp_f32 v20, v20\n" \
  "v_exp_f32 v21, v21\n" \
  "v_mfma_f32_32x32x16_bf16 v[32:47], v[160:163], v[136:139], v[32:47]\n" \
  "v_exp_f32 v22, v22\n" \
  "v_exp_f32 v23, v23\n" \
  "v_exp_f32 v24, v24\n" \
  "v_exp_f32 v25, v25\n" \
  "v_smfmac_f32_32x32x32_bf16 v[80:95], v[172:175], v[16:23], v188\n" \
  "v_exp_f32 v26, v26\n" \
  "v_exp_f32 v27, v27\n" \
  "v_exp_f32 v28, v28\n" \
  "v_exp_f32 v29, v29\n" \
  "v_mfma_f32_32x32x16_bf16 v[48:63], v[160:163], v[140:143], v[48:63]\n" \
  "v_exp_f32 v30, v30\n" \
  "v_exp_f32 v31, v31\n" \
  "s_cmp_lg_u32 s46, s47\n" \
  "s_cbranch_scc1 L_nobar2_%=\n" \
  "s_waitcnt vmcnt(0)\n" \
  "s_barrier\n" \
  "global_load_dword v203, %[pto], %[wsb] sc1\n" \
  "L_nobar2_%=:\n" \
  "v_exp_f32 v32, v32\n" \
  "v_exp_f32 v33, v33\n" \
  "v_smfmac_f32_32x32x32_bf16 v[80:95], v[176:179], v[24:31], v188\n" \
  "v_exp_f32 v34, v34\n" \
  "v_exp_f32 v35, v35\n" \
  "v_exp_f32 v36, v36\n" \
  "v_exp_f32 v37, v37\n" \
  "s_waitcnt lgkmcnt(0)\n" \
  "v_mfma_f32_32x32x4_2b_bf16 v[0:31], v[168:169], v[144:145], 0 cbsz:1 abid:1\n" \
  "v_exp_f32 v38, v38\n" \
  "v_exp_f32 v39, v39\n" \
  "v_exp_f32 v40, v40\n" \
  "v_exp_f32 v41, v41\n" \
  "v_exp_f32 v42, v42\n" \
  "v_exp_f32 v43, v43\n" \
  "v_exp_f32 v44, v44\n" \
  "v_exp_f32 v45, v45\n" \
  "v_smfmac_f32_32x32x32_bf16 v[96:111], v[172:175], v[32:39], v188\n" \
  "v_exp_f32 v46, v46\n" \
  "v_exp_f32 v47, v47\n" \
  "v_exp_f32 v48, v48\n" \
  "v_exp_f32 v49, v49\n" \
  "v_smfmac_f32_32x32x32_bf16 v[96:111], v[176:179], v[40:47], v188\n" \
  "v_exp_f32 v50, v50\n" \
  "v_exp_f32 v51, v51\n" \
  "v_exp_f32 v52, v52\n" \
  "v_exp_f32 v53, v53\n" \
  "v_mfma_f32_32x32x16_bf16 v[0:15], v[164:167], v[128:131], v[0:15]\n" \
  "v_exp_f32 v54, v54\n" \
  "v_exp_f32 v55, v55\n" \
  "v_exp_f32 v56, v56\n" \
  "v_exp_f32 v57, v57\n" \
  "v_smfmac_f32_32x32x32_bf16 v[112:127], v[172:175], v[48:55], v188\n" \
  "v_exp_f32 v58, v58\n" \
  "v_exp_f32 v59, v59\n" \
  "v_exp_f32 v60, v60\n" \
  "v_exp_f32 v61, v61\n" \
  "v_mfma_f32_32x32x16_bf16 v[16:31], v[164:167], v[132:135], v[16:31]\n" \
  "v_exp_f32 v62, v62\n" \
  "v_exp_f32 v63, v63\n" \
  "v_exp_f32 v0, v0\n" \
  "v_exp_f32 v1, v1\n" \
  "v_smfmac_f32_32x32x32_bf16 v[112:127], v[176:179], v[56:63], v188\n" \
  "ds_read_b128 v[160:163], v189 offset:2048\n" \
  "v_exp_f32 v2, v2\n" \
  "v_exp_f32 v3, v3\n" \
  "v_exp_f32 v4, v4\n" \
  "v_exp_f32 v5, v5\n" \
  "v_mfma_f32_32x32x4_2b_bf16 v[32:63], v[168:169], v[146:147], 0 cbsz:1 abid:1\n" \
  "v_exp_f32 v6, v6\n" \
  "v_exp_f32 v7, v7\n" \
  "v_exp_f32 v8, v8\n" \
  "v_exp_f32 v9, v9\n" \
  "v_exp_f32 v10, v10\n" \
  "v_exp_f32 v11, v11\n" \
  "v_exp_f32 v12, v12\n" \
  "v_exp_f32 v13, v13\n" \
  "v_smfmac_f32_32x32x32_bf16 v[64:79], v[180:183], v[0:7], v188\n" \
  "v_exp_f32 v14, v14\n" \
  "v_exp_f32 v15, v15\n" \
  "v_exp_f32 v16, v16\n" \
  "v_exp_f32 v17, v17\n" \
  "v_smfmac_f32_32x32x32_bf16 v[64:79], v[184:187], v[8:15], v188\n" \
  "s_mov_b64 exec, s[48:49]\n" \
  "ds_read_b128 v[172:175], v191 offset:7680\n" \
  "ds_read_b128 v[176:179], v191 offset:8320\n" \
  "s_mov_b64 exec, -1\n" \
  "v_exp_f32 v18, v18\n" \
  "v_exp_f32 v19, v19\n" \
  "v_exp_f32 v20, v20\n" \
  "v_exp_f32 v21, v21\n" \
  "v_mfma_f32_32x32x16_bf16 v[32:47], v[164:167], v[136:139], v[32:47]\n" \
  "v_exp_f32 v22, v22\n" \
  "v_exp_f32 v23, v23\n" \
  "v_exp_f32 v24, v24\n" \
  "v_exp_f32 v25, v25\n" \
  "v_smfmac_f32_32x32x32_bf16 v[80:95], v[180:183], v[16:23], v188\n" \
  "v_exp_f32 v26, v26\n" \
  "v_exp_f32 v27, v27\n" \
  "v_exp_f32 v28, v28\n" \
  "v_exp_f32 v29, v29\n" \
  "v_mfma_f32_32x32x16_bf16 v[48:63], v[164:167], v[140:143], v[48:63]\n" \
  "v_exp_f32 v30, v30\n" \
  "v_exp_f32 v31, v31\n" \
  "v_exp_f32 v32, v32\n" \
  "v_exp_f32 v33, v33\n" \
  "v_smfmac_f32_32x32x32_bf16 v[80:95], v[184:187], v[24:31], v188\n" \
  "v_exp_f32 v34, v34\n" \
  "v_exp_f32 v35, v35\n" \
  "v_exp_f32 v36, v36\n" \
  "v_exp_f32 v37, v37\n" \
  "s_waitcnt lgkmcnt(0)\n" \
  "v_mfma_f32_32x32x4_2b_bf16 v[0:31], v[170:171], v[144:145], 0 cbsz:1\n" \
  "v_exp_f32 v38, v38\n" \
  "v_exp_f32 v39, v39\n" \
  "v_exp_f32 v40, v40\n" \
  "v_exp_f32 v41, v41\n" \
  "v_exp_f32 v42, v42\n" \
  "v_exp_f32 v43, v43\n" \
  "v_exp_f32 v44, v44\n" \
  "v_exp_f32 v45, v45\n" \
  "v_smfmac_f32_32x32x32_bf16 v[96:111], v[180:183], v[32:39], v188\n" \
  "v_exp_f32 v46, v46\n" \
  "v_exp_f32 v47, v47\n" \
  "v_exp_f32 v48, v48\n" \
  "v_exp_f32 v49, v49\n" \
  "v_smfmac_f32_32x32x32_bf16 v[96:111], v[184:187], v[40:47], v188\n" \
  "v_exp_f32 v50, v50\n" \
  "v_exp_f32 v51, v51\n" \
  "v_exp_f32 v52, v52\n" \
  "v_exp_f32 v53, v53\n" \
  "v_mfma_f32_32x32x16_bf16 v[0:15], v[160:163], v[128:131], v[0:15]\n" \
  "v_exp_f32 v54, v54\n" \
  "v_exp_f32 v55, v55\n" \
  "v_exp_f32 v56, v56\n" \
  "v_exp_f32 v57, v57\n" \
  "v_smfmac_f32_32x32x32_bf16 v[112:127], v[180:183], v[48:55], v188\n" \
  "v_exp_f32 v58, v58\n" \
  "v_exp_f32 v59, v59\n" \
  "v_exp_f32 v60, v60\n" \
  "v_exp_f32 v61, v61\n" \
  "v_mfma_f32_32x32x16_bf16 v[16:31], v[160:163], v[132:135], v[16:31]\n" \
  "v_exp_f32 v62, v62\n" \
  "v_exp_f32 v63, v63\n" \
  "v_exp_f32 v0, v0\n" \
  "v_exp_f32 v1, v1\n" \
  "v_smfmac_f32_32x32x32_bf16 v[112:127], v[184:187], v[56:63], v188\n" \
  "ds_read_b128 v[164:167], v189 offset:3072\n" \
  "v_exp_f32 v2, v2\n" \
  "v_exp_f32 v3, v3\n" \
  "v_exp_f32 v4, v4\n" \
  "v_exp_f32 v5, v5\n" \
  "v_mfma_f32_32x32x4_2b_bf16 v[32:63], v[170:171], v[146:147], 0 cbsz:1\n" \
  "v_exp_f32 v6, v6\n" \
  "v_exp_f32 v7, v7\n" \
  "v_exp_f32 v8, v8\n" \
  "v_exp_f32 v9, v9\n" \
  "v_exp_f32 v10, v10\n" \
  "v_exp_f32 v11, v11\n" \
  "v_exp_f32 v12, v12\n" \
  "v_exp_f32 v13, v13\n" \
  "v_smfmac_f32_32x32x32_bf16 v[64:79], v[172:175], v[0:7], v188\n" \
  "v_exp_f32 v14, v14\n" \
  "v_exp_f32 v15, v15\n" \
  "v_exp_f32 v16, v16\n" \
  "v_exp_f32 v17, v17\n" \
  "v_smfmac_f32_32x32x32_bf16 v[64:79], v[176:179], v[8:15], v188\n" \
  "s_mov_b64 exec, s[48:49]\n" \
  "ds_read_b128 v[180:183], v191 offset:8960\n" \
  "ds_read_b128 v[184:187], v191 offset:9600\n" \
  "s_mov_b64 exec, -1\n" \
  "v_exp_f32 v18, v18\n" \
  "v_exp_f32 v19, v19\n" \
  "v_exp_f32 v20, v20\n" \
  "v_exp_f32 v21, v21\n" \
  "v_mfma_f32_32x32x16_bf16 v[32:47], v[160:163], v[136:139], v[32:47]\n" \
  "v_exp_f32 v22, v22\n" \
  "v_exp_f32 v23, v23\n" \
  "v_exp_f32 v24, v24\n" \
  "v_exp_f32 v25, v25\n" \
  "v_smfmac_f32_32x32x32_bf16 v[80:95], v[172:175], v[16:23], v188\n" \
  "v_exp_f32 v26, v26\n" \
  "v_exp_f32 v27, v27\n" \
  "v_exp_f32 v28, v28\n" \
  "v_exp_f32 v29, v29\n" \
  "v_mfma_f32_32x32x16_bf16 v[48:63], v[160:163], v[140:143], v[48:63]\n" \
  "v_exp_f32 v30, v30\n" \
  "v_exp_f32 v31, v31\n" \
  "v_exp_f32 v32, v32\n" \
  "v_exp_f32 v33, v33\n" \
  "v_smfmac_f32_32x32x32_bf16 v[80:95], v[176:179], v[24:31], v188\n" \
  "v_exp_f32 v34, v34\n" \
  "v_exp_f32 v35, v35\n" \
  "v_exp_f32 v36, v36\n" \
  "v_exp_f32 v37, v37\n" \
  "s_waitcnt lgkmcnt(0)\n" \
  "v_mfma_f32_32x32x4_2b_bf16 v[0:31], v[170:171], v[144:145], 0 cbsz:1 abid:1\n" \
  "v_exp_f32 v38, v38\n" \
  "v_exp_f32 v39, v39\n" \
  "v_exp_f32 v40, v40\n" \
  "v_exp_f32 v41, v41\n" \
  "v_exp_f32 v42, v42\n" \
  "v_exp_f32 v43, v43\n" \
  "v_exp_f32 v44, v44\n" \
  "v_exp_f32 v45, v45\n" \
  "v_smfmac_f32_32x32x32_bf16 v[96:111], v[172:175], v[32:39], v188\n" \
  "v_exp_f32 v46, v46\n" \
  "v_exp_f32 v47, v47\n" \
  "s_cmp_lg_u32 s46, s42\n" \
  "s_cbranch_scc1 L_nx0_%=\n" \
  "s_barrier\n" \
  "L_nx0_%=:\n" \
  "v_exp_f32 v48, v48\n" \
  "v_exp_f32 v49, v49\n" \
  "v_smfmac_f32_32x32x32_bf16 v[96:111], v[176:179], v[40:47], v188\n" \
  "v_exp_f32 v50, v50\n" \
  "v_exp_f32 v51, v51\n" \
  "v_exp_f32 v52, v52\n" \
  "v_exp_f32 v53, v53\n" \
  "v_mfma_f32_32x32x16_bf16 v[0:15], v[164:167], v[128:131], v[0:15]\n" \
  "v_exp_f32 v54, v54\n" \
  "v_exp_f32 v55, v55\n" \
  "v_exp_f32 v56, v56\n" \
  "v_exp_f32 v57, v57\n" \
  "v_smfmac_f32_32x32x32_bf16 v[112:127], v[172:175], v[48:55], v188\n" \
  "v_exp_f32 v58, v58\n" \
  "v_exp_f32 v59, v59\n" \
  "v_exp_f32 v60, v60\n" \
  "v_exp_f32 v61, v61\n" \
  "v_mfma_f32_32x32x16_bf16 v[16:31], v[164:167], v[132:135], v[16:31]\n" \
  "v_exp_f32 v62, v62\n" \
  "v_exp_f32 v63, v63\n" \
  "s_cmp_lg_u32 s46, s45\n" \
  "s_cbranch_scc1 L_nobar_%=\n" \
  "s_waitcnt vmcnt(0)\n" \
  "s_barrier\n" \
  "global_load_dword v203, %[pto], %[wsb] sc1\n" \
  "L_nobar_%=:\n" \
  "v_exp_f32 v0, v0\n" \
  "v_exp_f32 v1, v1\n" \
  "v_smfmac_f32_32x32x32_bf16 v[112:127], v[176:179], v[56:63], v188\n" \
  "ds_read_b128 v[160:163], v189 offset:10240\n" \
  "v_exp_f32 v2, v2\n" \
  "v_exp_f32 v3, v3\n" \
  "v_exp_f32 v4, v4\n" \
  "v_exp_f32 v5, v5\n" \
  "v_mfma_f32_32x32x4_2b_bf16 v[32:63], v[170:171], v[146:147], 0 cbsz:1 abid:1\n" \
  "ds_read_b64 v[168:169], v190 offset:14336\n" \
  "v_exp_f32 v6, v6\n" \
  "v_exp_f32 v7, v7\n" \
  "v_exp_f32 v8, v8\n" \
  "v_exp_f32 v9, v9\n" \
  "v_exp_f32 v10, v10\n" \
  "v_exp_f32 v11, v11\n" \
  "v_exp_f32 v12, v12\n" \
  "v_exp_f32 v13, v13\n" \
  "v_smfmac_f32_32x32x32_bf16 v[64:79], v[180:183], v[0:7], v188\n" \
  "v_exp_f32 v14, v14\n" \
  "v_exp_f32 v15, v15\n" \
  "v_exp_f32 v16, v16\n" \
  "v_exp_f32 v17, v17\n" \
  "v_smfmac_f32_32x32x32_bf16 v[64:79], v[184:187], v[8:15], v188\n" \
  "s_mov_b64 exec, s[48:49]\n" \
  "ds_read_b128 v[172:175], v191 offset:15360\n" \
  "ds_read_b128 v[176:179], v191 offset:16000\n" \
  "s_mov_b64 exec, -1\n" \
  "v_exp_f32 v18, v18\n" \
  "v_exp_f32 v19, v19\n" \
  "v_exp_f32 v20, v20\n" \
  "v_exp_f32 v21, v21\n" \
  "v_mfma_f32_32x32x16_bf16 v[32:47], v[164:167], v[136:139], v[32:47]\n" \
  "v_exp_f32 v22, v22\n" \
  "v_exp_f32 v23, v23\n" \
  "v_exp_f32 v24, v24\n" \
  "v_exp_f32 v25, v25\n" \
  "v_smfmac_f32_32x32x32_bf16 v[80:95], v[180:183], v[16:23], v188\n" \
  "v_exp_f32 v26, v26\n" \
  "v_exp_f32 v27, v27\n" \
  "v_exp_f32 v28, v28\n" \
  "v_exp_f32 v29, v29\n" \
  "v_mfma_f32_32x32x16_bf16 v[48:63], v[164:167], v[140:143], v[48:63]\n" \
  "v_exp_f32 v30, v30\n" \
  "v_exp_f32 v31, v31\n" \
  "v_exp_f32 v32, v32\n" \
  "v_exp_f32 v33, v33\n" \
  "v_smfmac_f32_32x32x32_bf16 v[80:95], v[184:187], v[24:31], v188\n" \
  "v_exp_f32 v34, v34\n" \
  "v_exp_f32 v35, v35\n" \
  "v_exp_f32 v36, v36\n" \
  "v_exp_f32 v37, v37\n" \
  "s_waitcnt lgkmcnt(0)\n" \
  "v_mfma_f32_32x32x4_2b_bf16 v[0:31], v[168:169], v[144:145], 0 cbsz:1\n" \
  "v_exp_f32 v38, v38\n" \
  "v_exp_f32 v39, v39\n" \
  "v_exp_f32 v40, v40\n" \
  "v_exp_f32 v41, v41\n" \
  "v_exp_f32 v42, v42\n" \
  "v_exp_f32 v43, v43\n" \
  "v_exp_f32 v44, v44\n" \
  "v_exp_f32 v45, v45\n" \
  "v_smfmac_f32_32x32x32_bf16 v[96:111], v[180:183], v[32:39], v188\n" \
  "v_exp_f32 v46, v46\n" \
  "v_exp_f32 v47, v47\n" \
  "v_exp_f32 v48, v48\n" \
  "v_exp_f32 v49, v49\n" \
  "v_smfmac_f32_32x32x32_bf16 v[96:111], v[184:187], v[40:47], v188\n" \
  "v_exp_f32 v50, v50\n" \
  "v_exp_f32 v51, v51\n" \
  "v_exp_f32 v52, v52\n" \
  "v_exp_f32 v53, v53\n" \
  "v_mfma_f32_32x32x16_bf16 v[0:15], v[160:163], v[128:131], v[0:15]\n" \
  "v_exp_f32 v54, v54\n" \
  "v_exp_f32 v55, v55\n" \
  "v_exp_f32 v56, v56\n" \
  "v_exp_f32 v57, v57\n" \
  "v_smfmac_f32_32x32x32_bf16 v[112:127], v[180:183], v[48:55], v188\n" \
  "v_add_u32 v189, 0x2800, v189\n" \
  "v_add_u32 v190, 0x2800, v190\n" \
  "v_add_u32 v191, 0x2800, v191\n" \
  "v_exp_f32 v58, v58\n" \
  "v_exp_f32 v59, v59\n" \
  "v_exp_f32 v60, v60\n" \
  "v_exp_f32 v61, v61\n" \
  "v_mfma_f32_32x32x16_bf16 v[16:31], v[160:163], v[132:135], v[16:31]\n" \
  "v_exp_f32 v62, v62\n" \
  "v_exp_f32 v63, v63\n" \
  "s_add_u32 s46, s46, 1\n" \
  "s_cmp_lg_u32 s46, 7\n" \
  "s_cbranch_scc1 L_loop_%=\n" \
  "v_exp_f32 v0, v0\n" \
  "v_exp_f32 v1, v1\n" \
  "v_smfmac_f32_32x32x32_bf16 v[112:127], v[184:187], v[56:63], v188\n" \
  "ds_read_b128 v[164:167], v189 offset:1024\n" \
  "v_exp_f32 v2, v2\n" \
  "v_exp_f32 v3, v3\n" \
  "v_exp_f32 v4, v4\n" \
  "v_exp_f32 v5, v5\n" \
  "v_mfma_f32_32x32x4_2b_bf16 v[32:63], v[168:169], v[146:147], 0 cbsz:1\n" \
  "ds_read_b64 v[170:171], v190 offset:4608\n" \
  "v_exp_f32 v6, v6\n" \
  "v_exp_f32 v7, v7\n" \
  "v_exp_f32 v8, v8\n" \
  "v_exp_f32 v9, v9\n" \
  "v_exp_f32 v10, v10\n" \
  "v_exp_f32 v11, v11\n" \
  "v_exp_f32 v12, v12\n" \
  "v_exp_f32 v13, v13\n" \
  "v_smfmac_f32_32x32x32_bf16 v[64:79], v[172:175], v[0:7], v188\n" \
  "v_exp_f32 v14, v14\n" \
  "v_exp_f32 v15, v15\n" \
  "s_cmp_eq_u32 %[hf], 0\n" \
  "s_cbranch_scc1 L_nox_%=\n" \
  "s_barrier\n" \
  "L_nox_%=:\n" \
  "v_exp_f32 v16, v16\n" \
  "v_exp_f32 v17, v17\n" \
  "v_smfmac_f32_32x32x32_bf16 v[64:79], v[176:179], v[8:15], v188\n" \
  "s_mov_b64 exec, s[48:49]\n" \
  "ds_read_b128 v[180:183], v191 offset:6400\n" \
  "ds_read_b128 v[184:187], v191 offset:7040\n" \
  "s_mov_b64 exec, -1\n" \
  "v_exp_f32 v18, v18\n" \
  "v_exp_f32 v19, v19\n" \
  "v_exp_f32 v20, v20\n" \
  "v_exp_f32 v21, v21\n" \
  "v_mfma_f32_32x32x16_bf16 v[32:47], v[160:163], v[136:139], v[32:47]\n" \
  "v_exp_f32 v22, v22\n" \
  "v_exp_f32 v23, v23\n" \
  "v_exp_f32 v24, v24\n" \
  "v_exp_f32 v25, v25\n" \
  "v_smfmac_f32_32x32x32_bf16 v[80:95], v[172:175], v[16:23], v188\n" \
  "v_exp_f32 v26, v26\n" \
  "v_exp_f32 v27, v27\n" \
  "v_exp_f32 v28, v28\n" \
  "v_exp_f32 v29, v29\n" \
  "v_mfma_f32_32x32x16_bf16 v[48:63], v[160:163], v[140:143], v[48:63]\n" \
  "v_exp_f32 v30, v30\n" \
  "v_exp_f32 v31, v31\n" \
  "v_exp_f32 v32, v32\n" \
  "v_exp_f32 v33, v33\n" \
  "v_smfmac_f32_32x32x32_bf16 v[80:95], v[176:179], v[24:31], v188\n" \
  "v_exp_f32 v34, v34\n" \
  "v_exp_f32 v35, v35\n" \
  "v_exp_f32 v36, v36\n" \
  "v_exp_f32 v37, v37\n" \
  "s_waitcnt lgkmcnt(0)\n" \
  "v_mfma_f32_32x32x4_2b_bf16 v[0:31], v[168:169], v[144:145], 0 cbsz:1 abid:1\n" \
  "v_exp_f32 v38, v38\n" \
  "v_exp_f32 v39, v39\n" \
  "v_exp_f32 v40, v40\n" \
  "v_exp_f32 v41, v41\n" \
  "v_exp_f32 v42, v42\n" \
  "v_exp_f32 v43, v43\n" \
  "v_exp_f32 v44, v44\n" \
  "v_exp_f32 v45, v45\n" \
  "v_smfmac_f32_32x32x32_bf16 v[96:111], v[172:175], v[32:39], v188\n" \
  "v_exp_f32 v46, v46\n" \
  "v_exp_f32 v47, v47\n" \
  "v_exp_f32 v48, v48\n" \
  "v_exp_f32 v49, v49\n" \
  "v_smfmac_f32_32x32x32_bf16 v[96:111], v[176:179], v[40:47], v188\n" \
  "v_exp_f32 v50, v50\n" \
  "v_exp_f32 v51, v51\n" \
  "v_exp_f32 v52, v52\n" \
  "v_exp_f32 v53, v53\n" \
  "v_mfma_f32_32x32x16_bf16 v[0:15], v[164:167], v[128:131], v[0:15]\n" \
  "v_exp_f32 v54, v54\n" \
  "v_exp_f32 v55, v55\n" \
  "v_exp_f32 v56, v56\n" \
  "v_exp_f32 v57, v57\n" \
  "v_smfmac_f32_32x32x32_bf16 v[112:127], v[172:175], v[48:55], v188\n" \
  "v_exp_f32 v58, v58\n" \
  "v_exp_f32 v59, v59\n" \
  "v_exp_f32 v60, v60\n" \
  "v_exp_f32 v61, v61\n" \
  "v_mfma_f32_32x32x16_bf16 v[16:31], v[164:167], v[132:135], v[16:31]\n" \
  "v_exp_f32 v62, v62\n" \
  "v_exp_f32 v63, v63\n" \
  "v_exp_f32 v0, v0\n" \
  "v_exp_f32 v1, v1\n" \
  "v_smfmac_f32_32x32x32_bf16 v[112:127], v[176:179], v[56:63], v188\n" \
  "ds_read_b128 v[160:163], v189 offset:2048\n" \
  "v_exp_f32 v2, v2\n" \
  "v_exp_f32 v3, v3\n" \
  "v_exp_f32 v4, v4\n" \
  "v_exp_f32 v5, v5\n" \
  "v_mfma_f32_32x32x4_2b_bf16 v[32:63], v[168:169], v[146:147], 0 cbsz:1 abid:1\n" \
  "v_exp_f32 v6, v6\n" \
  "v_exp_f32 v7, v7\n" \
  "v_exp_f32 v8, v8\n" \
  "v_exp_f32 v9, v9\n" \
  "v_exp_f32 v10, v10\n" \
  "v_exp_f32 v11, v11\n" \
  "v_exp_f32 v12, v12\n" \
  "v_exp_f32 v13, v13\n" \
  "v_smfmac_f32_32x32x32_bf16 v[64:79], v[180:183], v[0:7], v188\n" \
  "v_exp_f32 v14, v14\n" \
  "v_exp_f32 v15, v15\n" \
  "v_exp_f32 v16, v16\n" \
  "v_exp_f32 v17, v17\n" \
  "v_smfmac_f32_32x32x32_bf16 v[64:79], v[184:187], v[8:15], v188\n" \
  "s_mov_b64 exec, s[48:49]\n" \
  "ds_read_b128 v[172:175], v191 offset:7680\n" \
  "ds_read_b128 v[176:179], v191 offset:8320\n" \
  "s_mov_b64 exec, -1\n" \
  "v_exp_f32 v18, v18\n" \
  "v_exp_f32 v19, v19\n" \
  "v_exp_f32 v20, v20\n" \
  "v_exp_f32 v21, v21\n" \
  "v_mfma_f32_32x32x16_bf16 v[32:47], v[164:167], v[136:139], v[32:47]\n" \
  "v_exp_f32 v22, v22\n" \
  "v_exp_f32 v23, v23\n" \
  "v_exp_f32 v24, v24\n" \
  "v_exp_f32 v25, v25\n" \
  "v_smfmac_f32_32x32x32_bf16 v[80:95], v[180:183], v[16:23], v188\n" \
  "v_exp_f32 v26, v26\n" \
  "v_exp_f32 v27, v27\n" \
  "v_exp_f32 v28, v28\n" \
  "v_exp_f32 v29, v29\n" \
  "v_mfma_f32_32x32x16_bf16 v[48:63], v[164:167], v[140:143], v[48:63]\n" \
  "v_exp_f32 v30, v30\n" \
  "v_exp_f32 v31, v31\n" \
  "s_cmp_eq_u32 %[hf], 0\n" \
  "s_cbranch_scc1 L_nox3_%=\n" \
  "s_barrier\n" \
  "L_nox3_%=:\n" \
  "v_exp_f32 v32, v32\n" \
  "v_exp_f32 v33, v33\n" \
  "v_smfmac_f32_32x32x32_bf16 v[80:95], v[184:187], v[24:31], v188\n" \
  "v_exp_f32 v34, v34\n" \
  "v_exp_f32 v35, v35\n" \
  "v_exp_f32 v36, v36\n" \
  "v_exp_f32 v37, v37\n" \
  "s_waitcnt lgkmcnt(0)\n" \
  "v_mfma_f32_32x32x4_2b_bf16 v[0:31], v[170:171], v[144:145], 0 cbsz:1\n" \
  "ds_read_b128 v[202:205], v200 offset:0\n" \
  "ds_read_b128 v[206:209], v200 offset:1024\n" \
  "ds_read_b128 v[210:213], v200 offset:2048\n" \
  "v_exp_f32 v38, v38\n" \
  "v_exp_f32 v39, v39\n" \
  "v_exp_f32 v40, v40\n" \
  "v_exp_f32 v41, v41\n" \
  "v_exp_f32 v42, v42\n" \
  "v_exp_f32 v43, v43\n" \
  "v_exp_f32 v44, v44\n" \
  "v_exp_f32 v45, v45\n" \
  "v_smfmac_f32_32x32x32_bf16 v[96:111], v[180:183], v[32:39], v188\n" \
  "v_exp_f32 v46, v46\n" \
  "v_exp_f32 v47, v47\n" \
  "v_exp_f32 v48, v48\n" \
  "v_exp_f32 v49, v49\n" \
  "v_smfmac_f32_32x32x32_bf16 v[96:111], v[184:187], v[40:47], v188\n" \
  "ds_read_b128 v[214:217], v200 offset:3072\n" \
  "ds_read_b128 v[218:221], v200 offset:4096\n" \
  "v_exp_f32 v50, v50\n" \
  "v_exp_f32 v51, v51\n" \
  "v_exp_f32 v52, v52\n" \
  "v_exp_f32 v53, v53\n" \
  "v_mfma_f32_32x32x16_bf16 v[0:15], v[160:163], v[128:131], v[0:15]\n" \
  "ds_read_b128 v[222:225], v200 offset:5120\n" \
  "v_exp_f32 v54, v54\n" \
  "v_exp_f32 v55, v55\n" \
  "v_exp_f32 v56, v56\n" \
  "v_exp_f32 v57, v57\n" \
  "v_smfmac_f32_32x32x32_bf16 v[112:127], v[180:183], v[48:55], v188\n" \
  "v_exp_f32 v58, v58\n" \
  "v_exp_f32 v59, v59\n" \
  "v_exp_f32 v60, v60\n" \
  "v_exp_f32 v61, v61\n" \
  "v_mfma_f32_32x32x16_bf16 v[16:31], v[160:163], v[132:135], v[16:31]\n" \
  "v_exp_f32 v62, v62\n" \
  "v_exp_f32 v63, v63\n" \
  "v_exp_f32 v0, v0\n" \
  "v_exp_f32 v1, v1\n" \
  "v_smfmac_f32_32x32x32_bf16 v[112:127], v[184:187], v[56:63], v188\n" \
  "ds_read_b128 v[164:167], v189 offset:3072\n" \
  "ds_read_b128 v[226:229], v201 offset:0\n" \
  "ds_read_b128 v[230:233], v201 offset:1024\n" \
  "v_exp_f32 v2, v2\n" \
  "v_exp_f32 v3, v3\n" \
  "v_exp_f32 v4, v4\n" \
  "v_exp_f32 v5, v5\n" \
  "v_mfma_f32_32x32x4_2b_bf16 v[32:63], v[170:171], v[146:147], 0 cbsz:1\n" \
  "ds_read_b128 v[234:237], v201 offset:2048\n" \
  "v_exp_f32 v6, v6\n" \
  "v_exp_f32 v7, v7\n" \
  "v_exp_f32 v8, v8\n" \
  "v_exp_f32 v9, v9\n" \
  "v_exp_f32 v10, v10\n" \
  "v_exp_f32 v11, v11\n" \
  "v_exp_f32 v12, v12\n" \
  "v_exp_f32 v13, v13\n" \
  "v_smfmac_f32_32x32x32_bf16 v[64:79], v[172:175], v[0:7], v188\n" \
  "v_exp_f32 v14, v14\n" \
  "v_exp_f32 v15, v15\n" \
  "v_exp_f32 v16, v16\n" \
  "v_exp_f32 v17, v17\n" \
  "v_smfmac_f32_32x32x32_bf16 v[64:79], v[176:179], v[8:15], v188\n" \
  "s_mov_b64 exec, s[48:49]\n" \
  "ds_read_b128 v[180:183], v191 offset:8960\n" \
  "ds_read_b128 v[184:187], v191 offset:9600\n" \
  "s_mov_b64 exec, -1\n" \
  "ds_read_b128 v[238:241], v201 offset:3072\n" \
  "ds_read_b128 v[242:245], v201 offset:4096\n" \
  "v_exp_f32 v18, v18\n" \
  "v_exp_f32 v19, v19\n" \
  "v_exp_f32 v20, v20\n" \
  "v_exp_f32 v21, v21\n" \
  "v_mfma_f32_32x32x16_bf16 v[32:47], v[160:163], v[136:139], v[32:47]\n" \
  "ds_read_b128 v[246:249], v201 offset:5120\n" \
  "v_exp_f32 v22, v22\n" \
  "v_exp_f32 v23, v23\n" \
  "v_exp_f32 v24, v24\n" \
  "v_exp_f32 v25, v25\n" \
  "v_smfmac_f32_32x32x32_bf16 v[80:95], v[172:175], v[16:23], v188\n" \
  "v_exp_f32 v26, v26\n" \
  "v_exp_f32 v27, v27\n" \
  "v_exp_f32 v28, v28\n" \
  "v_exp_f32 v29, v29\n" \
  "v_mfma_f32_32x32x16_bf16 v[48:63], v[160:163], v[140:143], v[48:63]\n" \
  "v_exp_f32 v30, v30\n" \
  "v_exp_f32 v31, v31\n" \
  "v_exp_f32 v32, v32\n" \
  "v_exp_f32 v33, v33\n" \
  "v_smfmac_f32_32x32x32_bf16 v[80:95], v[176:179], v[24:31], v188\n" \
  "v_exp_f32 v34, v34\n" \
  "v_exp_f32 v35, v35\n" \
  "v_exp_f32 v36, v36\n" \
  "v_exp_f32 v37, v37\n" \
  "s_waitcnt lgkmcnt(0)\n" \
  "v_mfma_f32_32x32x4_2b_bf16 v[0:31], v[170:171], v[144:145], 0 cbsz:1 abid:1\n" \
  "v_exp_f32 v38, v38\n" \
  "v_exp_f32 v39, v39\n" \
  "v_exp_f32 v40, v40\n" \
  "v_exp_f32 v41, v41\n" \
  "v_exp_f32 v42, v42\n" \
  "v_exp_f32 v43, v43\n" \
  "v_exp_f32 v44, v44\n" \
  "v_exp_f32 v45, v45\n" \
  "v_smfmac_f32_32x32x32_bf16 v[96:111], v[172:175], v[32:39], v188\n" \
  "v_exp_f32 v46, v46\n" \
  "v_exp_f32 v47, v47\n" \
  "v_exp_f32 v48, v48\n" \
  "v_exp_f32 v49, v49\n" \
  "v_smfmac_f32_32x32x32_bf16 v[96:111], v[176:179], v[40:47], v188\n" \
  "v_exp_f32 v50, v50\n" \
  "v_exp_f32 v51, v51\n" \
  "v_exp_f32 v52, v52\n" \
  "v_exp_f32 v53, v53\n" \
  "v_mfma_f32_32x32x16_bf16 v[0:15], v[164:167], v[128:131], v[0:15]\n" \
  "v_exp_f32 v54, v54\n" \
  "v_exp_f32 v55, v55\n" \
  "v_exp_f32 v56, v56\n" \
  "v_exp_f32 v57, v57\n" \
  "v_smfmac_f32_32x32x32_bf16 v[112:127], v[172:175], v[48:55], v188\n" \
  "v_exp_f32 v58, v58\n" \
  "v_exp_f32 v59, v59\n" \
  "v_exp_f32 v60, v60\n" \
  "v_exp_f32 v61, v61\n" \
  "v_mfma_f32_32x32x16_bf16 v[16:31], v[164:167], v[132:135], v[16:31]\n" \
  "v_exp_f32 v62, v62\n" \
  "v_exp_f32 v63, v63\n" \
  "s_setprio 0\n" \
  "v_exp_f32 v0, v0\n" \
  "v_exp_f32 v1, v1\n" \
  "v_smfmac_f32_32x32x32_bf16 v[112:127], v[176:179], v[56:63], v188\n" \
  "v_exp_f32 v2, v2\n" \
  "v_exp_f32 v3, v3\n" \
  "v_exp_f32 v4, v4\n" \
  "v_exp_f32 v5, v5\n" \
  "v_mfma_f32_32x32x4_2b_bf16 v[32:63], v[170:171], v[146:147], 0 cbsz:1 abid:1\n" \
  "v_exp_f32 v6, v6\n" \
  "v_exp_f32 v7, v7\n" \
  "v_exp_f32 v8, v8\n" \
  "v_exp_f32 v9, v9\n" \
  "v_exp_f32 v10, v10\n" \
  "v_exp_f32 v11, v11\n" \
  "v_exp_f32 v12, v12\n" \
  "v_exp_f32 v13, v13\n" \
  "v_smfmac_f32_32x32x32_bf16 v[64:79], v[180:183], v[0:7], v188\n" \
  "v_exp_f32 v14, v14\n" \
  "v_exp_f32 v15, v15\n" \
  "v_exp_f32 v16, v16\n" \
  "v_exp_f32 v17, v17\n" \
  "v_smfmac_f32_32x32x32_bf16 v[64:79], v[184:187], v[8:15], v188\n" \
  "v_exp_f32 v18, v18\n" \
  "v_exp_f32 v19, v19\n" \
  "v_exp_f32 v20, v20\n" \
  "v_exp_f32 v21, v21\n" \
  "v_mfma_f32_32x32x16_bf16 v[32:47], v[164:167], v[136:139], v[32:47]\n" \
  "v_exp_f32 v22, v22\n" \
  "v_exp_f32 v23, v23\n" \
  "v_exp_f32 v24, v24\n" \
  "v_exp_f32 v25, v25\n" \
  "v_smfmac_f32_32x32x32_bf16 v[80:95], v[180:183], v[16:23], v188\n" \
  "v_exp_f32 v26, v26\n" \
  "v_exp_f32 v27, v27\n" \
  "v_exp_f32 v28, v28\n" \
  "v_exp_f32 v29, v29\n" \
  "v_mfma_f32_32x32x16_bf16 v[48:63], v[164:167], v[140:143], v[48:63]\n" \
  "v_exp_f32 v30, v30\n" \
  "v_exp_f32 v31, v31\n" \
  "v_exp_f32 v32, v32\n" \
  "v_exp_f32 v33, v33\n" \
  "v_smfmac_f32_32x32x32_bf16 v[80:95], v[184:187], v[24:31], v188\n" \
  "v_exp_f32 v34, v34\n" \
  "v_exp_f32 v35, v35\n" \
  "v_exp_f32 v36, v36\n" \
  "v_exp_f32 v37, v37\n" \
  "s_waitcnt lgkmcnt(0)\n" \
  "v_exp_f32 v38, v38\n" \
  "v_exp_f32 v39, v39\n" \
  "v_exp_f32 v40, v40\n" \
  "v_exp_f32 v41, v41\n" \
  "v_exp_f32 v42, v42\n" \
  "v_exp_f32 v43, v43\n" \
  "v_exp_f32 v44, v44\n" \
  "v_exp_f32 v45, v45\n" \
  "v_smfmac_f32_32x32x32_bf16 v[96:111], v[180:183], v[32:39], v188\n" \
  "v_exp_f32 v46, v46\n" \
  "v_exp_f32 v47, v47\n" \
  "v_exp_f32 v48, v48\n" \
  "v_exp_f32 v49, v49\n" \
  "v_smfmac_f32_32x32x32_bf16 v[96:111], v[184:187], v[40:47], v188\n" \
  "v_exp_f32 v50, v50\n" \
  "v_exp_f32 v51, v51\n" \
  "v_exp_f32 v52, v52\n" \
  "v_exp_f32 v53, v53\n" \
  "v_exp_f32 v54, v54\n" \
  "v_exp_f32 v55, v55\n" \
  "v_exp_f32 v56, v56\n" \
  "v_exp_f32 v57, v57\n" \
  "v_smfmac_f32_32x32x32_bf16 v[112:127], v[180:183], v[48:55], v188\n" \
  "v_exp_f32 v58, v58\n" \
  "v_exp_f32 v59, v59\n" \
  "v_exp_f32 v60, v60\n" \
  "v_exp_f32 v61, v61\n" \
  "v_exp_f32 v62, v62\n" \
  "v_exp_f32 v63, v63\n" \
  "s_nop 1\n" \
  "v_smfmac_f32_32x32x32_bf16 v[112:127], v[184:187], v[56:63], v188\n" \
  "s_setprio 0\n" \
  "s_nop 15\n" \
  "s_waitcnt vmcnt(0)\n" \
  "s_cmp_eq_u32 %[hf], 0\n" \
  "s_cbranch_scc0 L_now_%=\n" \
  "s_barrier\n" \
  "ds_write_b128 v200, v[64:67] offset:0\n" \
  "ds_write_b128 v200, v[68:71] offset:1024\n" \
  "ds_write_b128 v200, v[72:75] offset:2048\n" \
  "ds_write_b128 v200, v[80:83] offset:3072\n" \
  "ds_write_b128 v200, v[84:87] offset:4096\n" \
  "ds_write_b128 v200, v[88:91] offset:5120\n" \
  "ds_write_b128 v201, v[96:99] offset:0\n" \
  "ds_write_b128 v201, v[100:103] offset:1024\n" \
  "ds_write_b128 v201, v[104:107] offset:2048\n" \
  "ds_write_b128 v201, v[112:115] offset:3072\n" \
  "ds_write_b128 v201, v[116:119] offset:4096\n" \
  "ds_write_b128 v201, v[120:123] offset:5120\n" \
  "s_waitcnt lgkmcnt(0)\n" \
  "s_barrier\n" \
  "s_branch L_end_%=\n" \
  "L_now_%=:\n" \
  "s_waitcnt lgkmcnt(0)\n" \
  "v_pk_add_f32 v[64:65], v[64:65], v[202:203]\n" \
  "v_pk_add_f32 v[66:67], v[66:67], v[204:205]\n" \
  "v_pk_add_f32 v[68:69], v[68:69], v[206:207]\n" \
  "v_pk_add_f32 v[70:71], v[70:71], v[208:209]\n" \
  "v_pk_add_f32 v[72:73], v[72:73], v[210:211]\n" \
  "v_pk_add_f32 v[74:75], v[74:75], v[212:213]\n" \
  "v_pk_add_f32 v[80:81], v[80:81], v[214:215]\n" \
  "v_pk_add_f32 v[82:83], v[82:83], v[216:217]\n" \
  "v_pk_add_f32 v[84:85], v[84:85], v[218:219]\n" \
  "v_pk_add_f32 v[86:87], v[86:87], v[220:221]\n" \
  "v_pk_add_f32 v[88:89], v[88:89], v[222:223]\n" \
  "v_pk_add_f32 v[90:91], v[90:91], v[224:225]\n" \
  "v_pk_add_f32 v[96:97], v[96:97], v[226:227]\n" \
  "v_pk_add_f32 v[98:99], v[98:99], v[228:229]\n" \
  "v_pk_add_f32 v[100:101], v[100:101], v[230:231]\n" \
  "v_pk_add_f32 v[102:103], v[102:103], v[232:233]\n" \
  "v_pk_add_f32 v[104:105], v[104:105], v[234:235]\n" \
  "v_pk_add_f32 v[106:107], v[106:107], v[236:237]\n" \
  "v_pk_add_f32 v[112:113], v[112:113], v[238:239]\n" \
  "v_pk_add_f32 v[114:115], v[114:115], v[240:241]\n" \
  "v_pk_add_f32 v[116:117], v[116:117], v[242:243]\n" \
  "v_pk_add_f32 v[118:119], v[118:119], v[244:245]\n" \
  "v_pk_add_f32 v[120:121], v[120:121], v[246:247]\n" \
  "v_pk_add_f32 v[122:123], v[122:123], v[248:249]\n" \
  "s_nop 1\n" \
  "L_end_%=:\n" \
  ""
#define ATTN_CLOBBERS "v0", "v1", "v2", "v3", "v4", "v5", "v6", "v7", "v8", "v9", "v10", "v11", "v12", "v13", "v14", "v15", "v16", "v17", "v18", "v19", "v20", "v21", "v22", "v23", "v24", "v25", "v26", "v27", "v28", "v29", "v30", "v31", "v32", "v33", "v34", "v35", "v36", "v37", "v38", "v39", "v40", "v41", "v42", "v43", "v44", "v45", "v46", "v47", "v48", "v49", "v50", "v51", "v52", "v53", "v54", "v55", "v56", "v57", "v58", "v59", "v60", "v61", "v62", "v63", "v128", "v129", "v130", "v131", "v132", "v133", "v134", "v135", "v136", "v137", "v138", "v139", "v140", "v141", "v142", "v143", "v144", "v145", "v146", "v147", "v148", "v149", "v150", "v151", "v152", "v153", "v154", "v155", "v156", "v157", "v158", "v159", "v160", "v161", "v162", "v163", "v164", "v165", "v166", "v167", "v168", "v169", "v170", "v171", "v172", "v173", "v174", "v175", "v176", "v177", "v178", "v179", "v180", "v181", "v182", "v183", "v184", "v185", "v186", "v187", "v188", "v189", "v190", "v191", "v192", "v193", "v194", "v195", "v196", "v197", "v198", "v199", "v200", "v201", "v202", "v203", "v204", "v205", "v206", "v207", "v208", "v209", "v210", "v211", "v212", "v213", "v214", "v215", "v216", "v217", "v218", "v219", "v220", "v221", "v222", "v223", "v224", "v225", "v226", "v227", "v228", "v229", "v230", "v231", "v232", "v233", "v234", "v235", "v236", "v237", "v238", "v239", "v240", "v241", "v242", "v243", "v244", "v245", "v246", "v247", "v248", "v249", "s40", "s41", "s42", "s43", "s44", "s45", "s46", "s47", "s48", "s49", "s50", "s51", "m0", "scc", "vcc", "memory"

#define QKV_ASM_T \
  "s_mov_b64 s[44:45], %[xg]\n" \
  "s_mov_b64 s[46:47], %[wg]\n" \
  "s_mov_b32 s48, %[rot3]\n" \
  "s_add_u32 s40, s44, s48\n" \
  "s_addc_u32 s41, s45, 0\n" \
  "s_mul_i32 s49, s48, 1920\n" \
  "s_add_u32 s42, s46, s49\n" \
  "s_addc_u32 s43, s47, 0\n" \
  "s_add_u32 m0, %[xd], 49152\n" \
  "s_nop 0\n" \
  "global_load_lds_dwordx4 %[xs0], s[40:41]\n" \
  "s_add_u32 m0, %[xd], 50176\n" \
  "s_nop 0\n" \
  "global_load_lds_dwordx4 %[xs1], s[40:41]\n" \
  "s_add_u32 m0, %[xd], 51200\n" \
  "s_nop 0\n" \
  "global_load_lds_dwordx4 %[xs2], s[40:41]\n" \
  "s_add_u32 m0, %[xd], 52224\n" \
  "s_nop 0\n" \
  "global_load_lds_dwordx4 %[xs3], s[40:41]\n" \
  "s_add_u32 m0, %[wd], 49152\n" \
  "s_nop 0\n" \
  "global_load_lds_dwordx4 %[ws0], s[42:43]\n" \
  "s_add_u32 m0, %[wd], 50176\n" \
  "s_nop 0\n" \
  "global_load_lds_dwordx4 %[ws1], s[42:43]\n" \
  "s_add_u32 s48, s48, 128\n" \
  "s_cmp_eq_u32 s48, 1280\n" \
  "s_cselect_b32 s48, 0, s48\n" \
  "s_add_u32 s40, s44, s48\n" \
  "s_addc_u32 s41, s45, 0\n" \
  "s_mul_i32 s49, s48, 1920\n" \
  "s_add_u32 s42, s46, s49\n" \
  "s_addc_u32 s43, s47, 0\n" \
  "v_add_u32 v128, 98304, %[xa0]\n" \
  "v_add_u32 v129, 98304, %[xa1]\n" \
  "v_add_u32 v130, 98304, %[wa0]\n" \
  "v_add_u32 v131, 98304, %[wa1]\n" \
  "v_add_u32 v132, 98304, %[wa2]\n" \
  "v_add_u32 v133, 98304, %[wa3]\n" \
  "s_waitcnt vmcnt(6)\n" \
  "s_barrier\n" \
  "ds_read_b64_tr_b16 v[96:97], %[wa0] offset:0\n" \
  "ds_read_b64_tr_b16 v[98:99], %[wa0] offset:1024\n" \
  "ds_read_b128 v[64:67], %[xa0] offset:0\n" \
  "ds_read_b64_tr_b16 v[100:101], %[wa1] offset:0\n" \
  "ds_read_b64_tr_b16 v[102:103], %[wa1] offset:1024\n" \
  "ds_read_b128 v[68:71], %[xa0] offset:2048\n" \
  "ds_read_b64_tr_b16 v[104:105], %[wa2] offset:0\n" \
  "ds_read_b64_tr_b16 v[106:107], %[wa2] offset:1024\n" \
  "ds_read_b128 v[72:75], %[xa0] offset:4096\n" \
  "ds_read_b64_tr_b16 v[108:109], %[wa3] offset:0\n" \
  "ds_read_b64_tr_b16 v[110:111], %[wa3] offset:1024\n" \
  "ds_read_b128 v[76:79], %[xa0] offset:6144\n" \
  "s_waitcnt lgkmcnt(0)\n" \
  "v_mfma_f32_16x16x32_bf16 v[0:3], v[96:99], v[64:67], 0\n" \
  "ds_read_b64_tr_b16 v[112:113], %[wa0] offset:8192\n" \
  "v_mfma_f32_16x16x32_bf16 v[4:7], v[100:103], v[64:67], 0\n" \
  "ds_read_b64_tr_b16 v[114:115], %[wa0] offset:9216\n" \
  "v_mfma_f32_16x16x32_bf16 v[8:11], v[104:107], v[64:67], 0\n" \
  "ds_read_b128 v[80:83], %[xa1] offset:0\n" \
  "v_mfma_f32_16x16x32_bf16 v[12:15], v[108:111], v[64:67], 0\n" \
  "ds_read_b64_tr_b16 v[116:117], %[wa1] offset:8192\n" \
  "v_mfma_f32_16x16x32_bf16 v[16:19], v[96:99], v[68:71], 0\n" \
  "ds_read_b64_tr_b16 v[118:119], %[wa1] offset:9216\n" \
  "s_add_u32 m0, %[xd], 98304\n" \
  "s_nop 0\n" \
  "global_load_lds_dwordx4 %[xs0], s[40:41]\n" \
  "v_mfma_f32_16x16x32_bf16 v[20:23], v[100:103], v[68:71], 0\n" \
  "ds_read_b128 v[84:87], %[xa1] offset:2048\n" \
  "v_mfma_f32_16x16x32_bf16 v[24:27], v[104:107], v[68:71], 0\n" \
  "ds_read_b64_tr_b16 v[120:121], %[wa2] offset:8192\n" \
  "s_add_u32 m0, %[xd], 99328\n" \
  "s_nop 0\n" \
  "global_load_lds_dwordx4 %[xs1], s[40:41]\n" \
  "v_mfma_f32_16x16x32_bf16 v[28:31], v[108:111], v[68:71], 0\n" \
  "ds_read_b64_tr_b16 v[122:123], %[wa2] offset:9216\n" \
  "v_mfma_f32_16x16x32_bf16 v[32:35], v[96:99], v[72:75], 0\n" \
  "ds_read_b128 v[88:91], %[xa1] offset:4096\n" \
  "s_add_u32 m0, %[xd], 100352\n" \
  "s_nop 0\n" \
  "global_load_lds_dwordx4 %[xs2], s[40:41]\n" \
  "v_mfma_f32_16x16x32_bf16 v[36:39], v[100:103], v[72:75], 0\n" \
  "ds_read_b64_tr_b16 v[124:125], %[wa3] offset:8192\n" \
  "v_mfma_f32_16x16x32_bf16 v[40:43], v[104:107], v[72:75], 0\n" \
  "ds_read_b64_tr_b16 v[126:127], %[wa3] offset:9216\n" \
  "s_add_u32 m0, %[xd], 101376\n" \
  "s_nop 0\n" \
  "global_load_lds_dwordx4 %[xs3], s[40:41]\n" \
  "v_mfma_f32_16x16x32_bf16 v[44:47], v[108:111], v[72:75], 0\n" \
  "ds_read_b128 v[92:95], %[xa1] offset:6144\n" \
  "v_mfma_f32_16x16x32_bf16 v[48:51], v[96:99], v[76:79], 0\n" \
  "s_add_u32 m0, %[wd], 98304\n" \
  "s_nop 0\n" \
  "global_load_lds_dwordx4 %[ws0], s[42:43]\n" \
  "v_mfma_f32_16x16x32_bf16 v[52:55], v[100:103], v[76:79], 0\n" \
  "v_mfma_f32_16x16x32_bf16 v[56:59], v[104:107], v[76:79], 0\n" \
  "s_add_u32 m0, %[wd], 99328\n" \
  "s_nop 0\n" \
  "global_load_lds_dwordx4 %[ws1], s[42:43]\n" \
  "s_add_u32 s48, s48, 128\n" \
  "s_cmp_eq_u32 s48, 1280\n" \
  "s_cselect_b32 s48, 0, s48\n" \
  "s_add_u32 s40, s44, s48\n" \
  "s_addc_u32 s41, s45, 0\n" \
  "s_mul_i32 s49, s48, 1920\n" \
  "s_add_u32 s42, s46, s49\n" \
  "s_addc_u32 s43, s47, 0\n" \
  "v_mfma_f32_16x16x32_bf16 v[60:63], v[108:111], v[76:79], 0\n" \
  "s_waitcnt lgkmcnt(0)\n" \
  "s_waitcnt vmcnt(6)\n" \
  "s_barrier\n" \
  "v_mfma_f32_16x16x32_bf16 v[0:3], v[112:115], v[80:83], v[0:3]\n" \
  "ds_read_b64_tr_b16 v[96:97], %[wa0] offset:49152\n" \
  "v_mfma_f32_16x16x32_bf16 v[4:7], v[116:119], v[80:83], v[4:7]\n" \
  "ds_read_b64_tr_b16 v[98:99], %[wa0] offset:50176\n" \
  "v_mfma_f32_16x16x32_bf16 v[8:11], v[120:123], v[80:83], v[8:11]\n" \
  "ds_read_b128 v[64:67], %[xa0] offset:49152\n" \
  "v_mfma_f32_16x16x32_bf16 v[12:15], v[124:127], v[80:83], v[12:15]\n" \
  "ds_read_b64_tr_b16 v[100:101], %[wa1] offset:49152\n" \
  "v_mfma_f32_16x16x32_bf16 v[16:19], v[112:115], v[84:87], v[16:19]\n" \
  "ds_read_b64_tr_b16 v[102:103], %[wa1] offset:50176\n" \
  "s_add_u32 m0, %[xd], 0\n" \
  "s_nop 0\n" \
  "global_load_lds_dwordx4 %[xs0], s[40:41]\n" \
  "v_mfma_f32_16x16x32_bf16 v[20:23], v[116:119], v[84:87], v[20:23]\n" \
  "ds_read_b128 v[68:71], %[xa0] offset:51200\n" \
  "v_mfma_f32_16x16x32_bf16 v[24:27], v[120:123], v[84:87], v[24:27]\n" \
  "ds_read_b64_tr_b16 v[104:105], %[wa2] offset:49152\n" \
  "s_add_u32 m0, %[xd], 1024\n" \
  "s_nop 0\n" \
  "global_load_lds_dwordx4 %[xs1], s[40:41]\n" \
  "v_mfma_f32_16x16x32_bf16 v[28:31], v[124:127], v[84:87], v[28:31]\n" \
  "ds_read_b64_tr_b16 v[106:107], %[wa2] offset:50176\n" \
  "v_mfma_f32_16x16x32_bf16 v[32:35], v[112:115], v[88:91], v[32:35]\n" \
  "ds_read_b128 v[72:75], %[xa0] offset:53248\n" \
  "s_add_u32 m0, %[xd], 2048\n" \
  "s_nop 0\n" \
  "global_load_lds_dwordx4 %[xs2], s[40:41]\n" \
  "v_mfma_f32_16x16x32_bf16 v[36:39], v[116:119], v[88:91], v[36:39]\n" \
  "ds_read_b64_tr_b16 v[108:109], %[wa3] offset:49152\n" \
  "v_mfma_f32_16x16x32_bf16 v[40:43], v[120:123], v[88:91], v[40:43]\n" \
  "ds_read_b64_tr_b16 v[110:111], %[wa3] offset:50176\n" \
  "s_add_u32 m0, %[xd], 3072\n" \
  "s_nop 0\n" \
  "global_load_lds_dwordx4 %[xs3], s[40:41]\n" \
  "v_mfma_f32_16x16x32_bf16 v[44:47], v[124:127], v[88:91], v[44:47]\n" \
  "ds_read_b128 v[76:79], %[xa0] offset:55296\n" \
  "v_mfma_f32_16x16x32_bf16 v[48:51], v[112:115], v[92:95], v[48:51]\n" \
  "s_add_u32 m0, %[wd], 0\n" \
  "s_nop 0\n" \
  "global_load_lds_dwordx4 %[ws0], s[42:43]\n" \
  "v_mfma_f32_16x16x32_bf16 v[52:55], v[116:119], v[92:95], v[52:55]\n" \
  "v_mfma_f32_16x16x32_bf16 v[56:59], v[120:123], v[92:95], v[56:59]\n" \
  "s_add_u32 m0, %[wd], 1024\n" \
  "s_nop 0\n" \
  "global_load_lds_dwordx4 %[ws1], s[42:43]\n" \
  "s_add_u32 s48, s48, 128\n" \
  "s_cmp_eq_u32 s48, 1280\n" \
  "s_cselect_b32 s48, 0, s48\n" \
  "s_add_u32 s40, s44, s48\n" \
  "s_addc_u32 s41, s45, 0\n" \
  "s_mul_i32 s49, s48, 1920\n" \
  "s_add_u32 s42, s46, s49\n" \
  "s_addc_u32 s43, s47, 0\n" \
  "v_mfma_f32_16x16x32_bf16 v[60:63], v[124:127], v[92:95], v[60:63]\n" \
  "s_waitcnt lgkmcnt(0)\n" \
  "v_mfma_f32_16x16x32_bf16 v[0:3], v[96:99], v[64:67], v[0:3]\n" \
  "ds_read_b64_tr_b16 v[112:113], %[wa0] offset:57344\n" \
  "v_mfma_f32_16x16x32_bf16 v[4:7], v[100:103], v[64:67], v[4:7]\n" \
  "ds_read_b64_tr_b16 v[114:115], %[wa0] offset:58368\n" \
  "v_mfma_f32_16x16x32_bf16 v[8:11], v[104:107], v[64:67], v[8:11]\n" \
  "ds_read_b128 v[80:83], %[xa1] offset:49152\n" \
  "v_mfma_f32_16x16x32_bf16 v[12:15], v[108:111], v[64:67], v[12:15]\n" \
  "ds_read_b64_tr_b16 v[116:117], %[wa1] offset:57344\n" \
  "v_mfma_f32_16x16x32_bf16 v[16:19], v[96:99], v[68:71], v[16:19]\n" \
  "ds_read_b64_tr_b16 v[118:119], %[wa1] offset:58368\n" \
  "v_mfma_f32_16x16x32_bf16 v[20:23], v[100:103], v[68:71], v[20:23]\n" \
  "ds_read_b128 v[84:87], %[xa1] offset:51200\n" \
  "v_mfma_f32_16x16x32_bf16 v[24:27], v[104:107], v[68:71], v[24:27]\n" \
  "ds_read_b64_tr_b16 v[120:121], %[wa2] offset:57344\n" \
  "v_mfma_f32_16x16x32_bf16 v[28:31], v[108:111], v[68:71], v[28:31]\n" \
  "ds_read_b64_tr_b16 v[122:123], %[wa2] offset:58368\n" \
  "v_mfma_f32_16x16x32_bf16 v[32:35], v[96:99], v[72:75], v[32:35]\n" \
  "ds_read_b128 v[88:91], %[xa1] offset:53248\n" \
  "v_mfma_f32_16x16x32_bf16 v[36:39], v[100:103], v[72:75], v[36:39]\n" \
  "ds_read_b64_tr_b16 v[124:125], %[wa3] offset:57344\n" \
  "v_mfma_f32_16x16x32_bf16 v[40:43], v[104:107], v[72:75], v[40:43]\n" \
  "ds_read_b64_tr_b16 v[126:127], %[wa3] offset:58368\n" \
  "v_mfma_f32_16x16x32_bf16 v[44:47], v[108:111], v[72:75], v[44:47]\n" \
  "ds_read_b128 v[92:95], %[xa1] offset:55296\n" \
  "v_mfma_f32_16x16x32_bf16 v[48:51], v[96:99], v[76:79], v[48:51]\n" \
  "v_mfma_f32_16x16x32_bf16 v[52:55], v[100:103], v[76:79], v[52:55]\n" \
  "v_mfma_f32_16x16x32_bf16 v[56:59], v[104:107], v[76:79], v[56:59]\n" \
  "v_mfma_f32_16x16x32_bf16 v[60:63], v[108:111], v[76:79], v[60:63]\n" \
  "s_waitcnt lgkmcnt(0)\n" \
  "s_waitcnt vmcnt(6)\n" \
  "s_barrier\n" \
  "v_mfma_f32_16x16x32_bf16 v[0:3], v[112:115], v[80:83], v[0:3]\n" \
  "ds_read_b64_tr_b16 v[96:97], v130 offset:0\n" \
  "v_mfma_f32_16x16x32_bf16 v[4:7], v[116:119], v[80:83], v[4:7]\n" \
  "ds_read_b64_tr_b16 v[98:99], v130 offset:1024\n" \
  "v_mfma_f32_16x16x32_bf16 v[8:11], v[120:123], v[80:83], v[8:11]\n" \
  "ds_read_b128 v[64:67], v128 offset:0\n" \
  "v_mfma_f32_16x16x32_bf16 v[12:15], v[124:127], v[80:83], v[12:15]\n" \
  "ds_read_b64_tr_b16 v[100:101], v131 offset:0\n" \
  "v_mfma_f32_16x16x32_bf16 v[16:19], v[112:115], v[84:87], v[16:19]\n" \
  "ds_read_b64_tr_b16 v[102:103], v131 offset:1024\n" \
  "s_add_u32 m0, %[xd], 49152\n" \
  "s_nop 0\n" \
  "global_load_lds_dwordx4 %[xs0], s[40:41]\n" \
  "v_mfma_f32_16x16x32_bf16 v[20:23], v[116:119], v[84:87], v[20:23]\n" \
  "ds_read_b128 v[68:71], v128 offset:2048\n" \
  "v_mfma_f32_16x16x32_bf16 v[24:27], v[120:123], v[84:87], v[24:27]\n" \
  "ds_read_b64_tr_b16 v[104:105], v132 offset:0\n" \
  "s_add_u32 m0, %[xd], 50176\n" \
  "s_nop 0\n" \
  "global_load_lds_dwordx4 %[xs1], s[40:41]\n" \
  "v_mfma_f32_16x16x32_bf16 v[28:31], v[124:127], v[84:87], v[28:31]\n" \
  "ds_read_b64_tr_b16 v[106:107], v132 offset:1024\n" \
  "v_mfma_f32_16x16x32_bf16 v[32:35], v[112:115], v[88:91], v[32:35]\n" \
  "ds_read_b128 v[72:75], v128 offset:4096\n" \
  "s_add_u32 m0, %[xd], 51200\n" \
  "s_nop 0\n" \
  "global_load_lds_dwordx4 %[xs2], s[40:41]\n" \
  "v_mfma_f32_16x16x32_bf16 v[36:39], v[116:119], v[88:91], v[36:39]\n" \
  "ds_read_b64_tr_b16 v[108:109], v133 offset:0\n" \
  "v_mfma_f32_16x16x32_bf16 v[40:43], v[120:123], v[88:91], v[40:43]\n" \
  "ds_read_b64_tr_b16 v[110:111], v133 offset:1024\n" \
  "s_add_u32 m0, %[xd], 52224\n" \
  "s_nop 0\n" \
  "global_load_lds_dwordx4 %[xs3], s[40:41]\n" \
  "v_mfma_f32_16x16x32_bf16 v[44:47], v[124:127], v[88:91], v[44:47]\n" \
  "ds_read_b128 v[76:79], v128 offset:6144\n" \
  "v_mfma_f32_16x16x32_bf16 v[48:51], v[112:115], v[92:95], v[48:51]\n" \
  "s_add_u32 m0, %[wd], 49152\n" \
  "s_nop 0\n" \
  "global_load_lds_dwordx4 %[ws0], s[42:43]\n" \
  "v_mfma_f32_16x16x32_bf16 v[52:55], v[116:119], v[92:95], v[52:55]\n" \
  "v_mfma_f32_16x16x32_bf16 v[56:59], v[120:123], v[92:95], v[56:59]\n" \
  "s_add_u32 m0, %[wd], 50176\n" \
  "s_nop 0\n" \
  "global_load_lds_dwordx4 %[ws1], s[42:43]\n" \
  "s_add_u32 s48, s48, 128\n" \
  "s_cmp_eq_u32 s48, 1280\n" \
  "s_cselect_b32 s48, 0, s48\n" \
  "s_add_u32 s40, s44, s48\n" \
  "s_addc_u32 s41, s45, 0\n" \
  "s_mul_i32 s49, s48, 1920\n" \
  "s_add_u32 s42, s46, s49\n" \
  "s_addc_u32 s43, s47, 0\n" \
  "v_mfma_f32_16x16x32_bf16 v[60:63], v[124:127], v[92:95], v[60:63]\n" \
  "s_waitcnt lgkmcnt(0)\n" \
  "v_mfma_f32_16x16x32_bf16 v[0:3], v[96:99], v[64:67], v[0:3]\n" \
  "ds_read_b64_tr_b16 v[112:113], v130 offset:8192\n" \
  "v_mfma_f32_16x16x32_bf16 v[4:7], v[100:103], v[64:67], v[4:7]\n" \
  "ds_read_b64_tr_b16 v[114:115], v130 offset:9216\n" \
  "v_mfma_f32_16x16x32_bf16 v[8:11], v[104:107], v[64:67], v[8:11]\n" \
  "ds_read_b128 v[80:83], v129 offset:0\n" \
  "v_mfma_f32_16x16x32_bf16 v[12:15], v[108:111], v[64:67], v[12:15]\n" \
  "ds_read_b64_tr_b16 v[116:117], v131 offset:8192\n" \
  "v_mfma_f32_16x16x32_bf16 v[16:19], v[96:99], v[68:71], v[16:19]\n" \
  "ds_read_b64_tr_b16 v[118:119], v131 offset:9216\n" \
  "v_mfma_f32_16x16x32_bf16 v[20:23], v[100:103], v[68:71], v[20:23]\n" \
  "ds_read_b128 v[84:87], v129 offset:2048\n" \
  "v_mfma_f32_16x16x32_bf16 v[24:27], v[104:107], v[68:71], v[24:27]\n" \
  "ds_read_b64_tr_b16 v[120:121], v132 offset:8192\n" \
  "v_mfma_f32_16x16x32_bf16 v[28:31], v[108:111], v[68:71], v[28:31]\n" \
  "ds_read_b64_tr_b16 v[122:123], v132 offset:9216\n" \
  "v_mfma_f32_16x16x32_bf16 v[32:35], v[96:99], v[72:75], v[32:35]\n" \
  "ds_read_b128 v[88:91], v129 offset:4096\n" \
  "v_mfma_f32_16x16x32_bf16 v[36:39], v[100:103], v[72:75], v[36:39]\n" \
  "ds_read_b64_tr_b16 v[124:125], v133 offset:8192\n" \
  "v_mfma_f32_16x16x32_bf16 v[40:43], v[104:107], v[72:75], v[40:43]\n" \
  "ds_read_b64_tr_b16 v[126:127], v133 offset:9216\n" \
  "v_mfma_f32_16x16x32_bf16 v[44:47], v[108:111], v[72:75], v[44:47]\n" \
  "ds_read_b128 v[92:95], v129 offset:6144\n" \
  "v_mfma_f32_16x16x32_bf16 v[48:51], v[96:99], v[76:79], v[48:51]\n" \
  "v_mfma_f32_16x16x32_bf16 v[52:55], v[100:103], v[76:79], v[52:55]\n" \
  "v_mfma_f32_16x16x32_bf16 v[56:59], v[104:107], v[76:79], v[56:59]\n" \
  "v_mfma_f32_16x16x32_bf16 v[60:63], v[108:111], v[76:79], v[60:63]\n" \
  "s_waitcnt lgkmcnt(0)\n" \
  "s_waitcnt vmcnt(6)\n" \
  "s_barrier\n" \
  "v_mfma_f32_16x16x32_bf16 v[0:3], v[112:115], v[80:83], v[0:3]\n" \
  "ds_read_b64_tr_b16 v[96:97], %[wa0] offset:0\n" \
  "v_mfma_f32_16x16x32_bf16 v[4:7], v[116:119], v[80:83], v[4:7]\n" \
  "ds_read_b64_tr_b16 v[98:99], %[wa0] offset:1024\n" \
  "v_mfma_f32_16x16x32_bf16 v[8:11], v[120:123], v[80:83], v[8:11]\n" \
  "ds_read_b128 v[64:67], %[xa0] offset:0\n" \
  "v_mfma_f32_16x16x32_bf16 v[12:15], v[124:127], v[80:83], v[12:15]\n" \
  "ds_read_b64_tr_b16 v[100:101], %[wa1] offset:0\n" \
  "v_mfma_f32_16x16x32_bf16 v[16:19], v[112:115], v[84:87], v[16:19]\n" \
  "ds_read_b64_tr_b16 v[102:103], %[wa1] offset:1024\n" \
  "s_add_u32 m0, %[xd], 98304\n" \
  "s_nop 0\n" \
  "global_load_lds_dwordx4 %[xs0], s[40:41]\n" \
  "v_mfma_f32_16x16x32_bf16 v[20:23], v[116:119], v[84:87], v[20:23]\n" \
  "ds_read_b128 v[68:71], %[xa0] offset:2048\n" \
  "v_mfma_f32_16x16x32_bf16 v[24:27], v[120:123], v[84:87], v[24:27]\n" \
  "ds_read_b64_tr_b16 v[104:105], %[wa2] offset:0\n" \
  "s_add_u32 m0, %[xd], 99328\n" \
  "s_nop 0\n" \
  "global_load_lds_dwordx4 %[xs1], s[40:41]\n" \
  "v_mfma_f32_16x16x32_bf16 v[28:31], v[124:127], v[84:87], v[28:31]\n" \
  "ds_read_b64_tr_b16 v[106:107], %[wa2] offset:1024\n" \
  "v_mfma_f32_16x16x32_bf16 v[32:35], v[112:115], v[88:91], v[32:35]\n" \
  "ds_read_b128 v[72:75], %[xa0] offset:4096\n" \
  "s_add_u32 m0, %[xd], 100352\n" \
  "s_nop 0\n" \
  "global_load_lds_dwordx4 %[xs2], s[40:41]\n" \
  "v_mfma_f32_16x16x32_bf16 v[36:39], v[116:119], v[88:91], v[36:39]\n" \
  "ds_read_b64_tr_b16 v[108:109], %[wa3] offset:0\n" \
  "v_mfma_f32_16x16x32_bf16 v[40:43], v[120:123], v[88:91], v[40:43]\n" \
  "ds_read_b64_tr_b16 v[110:111], %[wa3] offset:1024\n" \
  "s_add_u32 m0, %[xd], 101376\n" \
  "s_nop 0\n" \
  "global_load_lds_dwordx4 %[xs3], s[40:41]\n" \
  "v_mfma_f32_16x16x32_bf16 v[44:47], v[124:127], v[88:91], v[44:47]\n" \
  "ds_read_b128 v[76:79], %[xa0] offset:6144\n" \
  "v_mfma_f32_16x16x32_bf16 v[48:51], v[112:115], v[92:95], v[48:51]\n" \
  "s_add_u32 m0, %[wd], 98304\n" \
  "s_nop 0\n" \
  "global_load_lds_dwordx4 %[ws0], s[42:43]\n" \
  "v_mfma_f32_16x16x32_bf16 v[52:55], v[116:119], v[92:95], v[52:55]\n" \
  "v_mfma_f32_16x16x32_bf16 v[56:59], v[120:123], v[92:95], v[56:59]\n" \
  "s_add_u32 m0, %[wd], 99328\n" \
  "s_nop 0\n" \
  "global_load_lds_dwordx4 %[ws1], s[42:43]\n" \
  "s_add_u32 s48, s48, 128\n" \
  "s_cmp_eq_u32 s48, 1280\n" \
  "s_cselect_b32 s48, 0, s48\n" \
  "s_add_u32 s40, s44, s48\n" \
  "s_addc_u32 s41, s45, 0\n" \
  "s_mul_i32 s49, s48, 1920\n" \
  "s_add_u32 s42, s46, s49\n" \
  "s_addc_u32 s43, s47, 0\n" \
  "v_mfma_f32_16x16x32_bf16 v[60:63], v[124:127], v[92:95], v[60:63]\n" \
  "s_waitcnt lgkmcnt(0)\n" \
  "v_mfma_f32_16x16x32_bf16 v[0:3], v[96:99], v[64:67], v[0:3]\n" \
  "ds_read_b64_tr_b16 v[112:113], %[wa0] offset:8192\n" \
  "v_mfma_f32_16x16x32_bf16 v[4:7], v[100:103], v[64:67], v[4:7]\n" \
  "ds_read_b64_tr_b16 v[114:115], %[wa0] offset:9216\n" \
  "v_mfma_f32_16x16x32_bf16 v[8:11], v[104:107], v[64:67], v[8:11]\n" \
  "ds_read_b128 v[80:83], %[xa1] offset:0\n" \
  "v_mfma_f32_16x16x32_bf16 v[12:15], v[108:111], v[64:67], v[12:15]\n" \
  "ds_read_b64_tr_b16 v[116:117], %[wa1] offset:8192\n" \
  "v_mfma_f32_16x16x32_bf16 v[16:19], v[96:99], v[68:71], v[16:19]\n" \
  "ds_read_b64_tr_b16 v[118:119], %[wa1] offset:9216\n" \
  "v_mfma_f32_16x16x32_bf16 v[20:23], v[100:103], v[68:71], v[20:23]\n" \
  "ds_read_b128 v[84:87], %[xa1] offset:2048\n" \
  "v_mfma_f32_16x16x32_bf16 v[24:27], v[104:107], v[68:71], v[24:27]\n" \
  "ds_read_b64_tr_b16 v[120:121], %[wa2] offset:8192\n" \
  "v_mfma_f32_16x16x32_bf16 v[28:31], v[108:111], v[68:71], v[28:31]\n" \
  "ds_read_b64_tr_b16 v[122:123], %[wa2] offset:9216\n" \
  "v_mfma_f32_16x16x32_bf16 v[32:35], v[96:99], v[72:75], v[32:35]\n" \
  "ds_read_b128 v[88:91], %[xa1] offset:4096\n" \
  "v_mfma_f32_16x16x32_bf16 v[36:39], v[100:103], v[72:75], v[36:39]\n" \
  "ds_read_b64_tr_b16 v[124:125], %[wa3] offset:8192\n" \
  "v_mfma_f32_16x16x32_bf16 v[40:43], v[104:107], v[72:75], v[40:43]\n" \
  "ds_read_b64_tr_b16 v[126:127], %[wa3] offset:9216\n" \
  "v_mfma_f32_16x16x32_bf16 v[44:47], v[108:111], v[72:75], v[44:47]\n" \
  "ds_read_b128 v[92:95], %[xa1] offset:6144\n" \
  "v_mfma_f32_16x16x32_bf16 v[48:51], v[96:99], v[76:79], v[48:51]\n" \
  "v_mfma_f32_16x16x32_bf16 v[52:55], v[100:103], v[76:79], v[52:55]\n" \
  "v_mfma_f32_16x16x32_bf16 v[56:59], v[104:107], v[76:79], v[56:59]\n" \
  "v_mfma_f32_16x16x32_bf16 v[60:63], v[108:111], v[76:79], v[60:63]\n" \
  "s_waitcnt lgkmcnt(0)\n" \
  "s_waitcnt vmcnt(6)\n" \
  "s_barrier\n" \
  "v_mfma_f32_16x16x32_bf16 v[0:3], v[112:115], v[80:83], v[0:3]\n" \
  "ds_read_b64_tr_b16 v[96:97], %[wa0] offset:49152\n" \
  "v_mfma_f32_16x16x32_bf16 v[4:7], v[116:119], v[80:83], v[4:7]\n" \
  "ds_read_b64_tr_b16 v[98:99], %[wa0] offset:50176\n" \
  "v_mfma_f32_16x16x32_bf16 v[8:11], v[120:123], v[80:83], v[8:11]\n" \
  "ds_read_b128 v[64:67], %[xa0] offset:49152\n" \
  "v_mfma_f32_16x16x32_bf16 v[12:15], v[124:127], v[80:83], v[12:15]\n" \
  "ds_read_b64_tr_b16 v[100:101], %[wa1] offset:49152\n" \
  "v_mfma_f32_16x16x32_bf16 v[16:19], v[112:115], v[84:87], v[16:19]\n" \
  "ds_read_b64_tr_b16 v[102:103], %[wa1] offset:50176\n" \
  "s_add_u32 m0, %[xd], 0\n" \
  "s_nop 0\n" \
  "global_load_lds_dwordx4 %[xs0], s[40:41]\n" \
  "v_mfma_f32_16x16x32_bf16 v[20:23], v[116:119], v[84:87], v[20:23]\n" \
  "ds_read_b128 v[68:71], %[xa0] offset:51200\n" \
  "v_mfma_f32_16x16x32_bf16 v[24:27], v[120:123], v[84:87], v[24:27]\n" \
  "ds_read_b64_tr_b16 v[104:105], %[wa2] offset:49152\n" \
  "s_add_u32 m0, %[xd], 1024\n" \
  "s_nop 0\n" \
  "global_load_lds_dwordx4 %[xs1], s[40:41]\n" \
  "v_mfma_f32_16x16x32_bf16 v[28:31], v[124:127], v[84:87], v[28:31]\n" \
  "ds_read_b64_tr_b16 v[106:107], %[wa2] offset:50176\n" \
  "v_mfma_f32_16x16x32_bf16 v[32:35], v[112:115], v[88:91], v[32:35]\n" \
  "ds_read_b128 v[72:75], %[xa0] offset:53248\n" \
  "s_add_u32 m0, %[xd], 2048\n" \
  "s_nop 0\n" \
  "global_load_lds_dwordx4 %[xs2], s[40:41]\n" \
  "v_mfma_f32_16x16x32_bf16 v[36:39], v[116:119], v[88:91], v[36:39]\n" \
  "ds_read_b64_tr_b16 v[108:109], %[wa3] offset:49152\n" \
  "v_mfma_f32_16x16x32_bf16 v[40:43], v[120:123], v[88:91], v[40:43]\n" \
  "ds_read_b64_tr_b16 v[110:111], %[wa3] offset:50176\n" \
  "s_add_u32 m0, %[xd], 3072\n" \
  "s_nop 0\n" \
  "global_load_lds_dwordx4 %[xs3], s[40:41]\n" \
  "v_mfma_f32_16x16x32_bf16 v[44:47], v[124:127], v[88:91], v[44:47]\n" \
  "ds_read_b128 v[76:79], %[xa0] offset:55296\n" \
  "v_mfma_f32_16x16x32_bf16 v[48:51], v[112:115], v[92:95], v[48:51]\n" \
  "s_add_u32 m0, %[wd], 0\n" \
  "s_nop 0\n" \
  "global_load_lds_dwordx4 %[ws0], s[42:43]\n" \
  "v_mfma_f32_16x16x32_bf16 v[52:55], v[116:119], v[92:95], v[52:55]\n" \
  "v_mfma_f32_16x16x32_bf16 v[56:59], v[120:123], v[92:95], v[56:59]\n" \
  "s_add_u32 m0, %[wd], 1024\n" \
  "s_nop 0\n" \
  "global_load_lds_dwordx4 %[ws1], s[42:43]\n" \
  "s_add_u32 s48, s48, 128\n" \
  "s_cmp_eq_u32 s48, 1280\n" \
  "s_cselect_b32 s48, 0, s48\n" \
  "s_add_u32 s40, s44, s48\n" \
  "s_addc_u32 s41, s45, 0\n" \
  "s_mul_i32 s49, s48, 1920\n" \
  "s_add_u32 s42, s46, s49\n" \
  "s_addc_u32 s43, s47, 0\n" \
  "v_mfma_f32_16x16x32_bf16 v[60:63], v[124:127], v[92:95], v[60:63]\n" \
  "s_waitcnt lgkmcnt(0)\n" \
  "v_mfma_f32_16x16x32_bf16 v[0:3], v[96:99], v[64:67], v[0:3]\n" \
  "ds_read_b64_tr_b16 v[112:113], %[wa0] offset:57344\n" \
  "v_mfma_f32_16x16x32_bf16 v[4:7], v[100:103], v[64:67], v[4:7]\n" \
  "ds_read_b64_tr_b16 v[114:115], %[wa0] offset:58368\n" \
  "v_mfma_f32_16x16x32_bf16 v[8:11], v[104:107], v[64:67], v[8:11]\n" \
  "ds_read_b128 v[80:83], %[xa1] offset:49152\n" \
  "v_mfma_f32_16x16x32_bf16 v[12:15], v[108:111], v[64:67], v[12:15]\n" \
  "ds_read_b64_tr_b16 v[116:117], %[wa1] offset:57344\n" \
  "v_mfma_f32_16x16x32_bf16 v[16:19], v[96:99], v[68:71], v[16:19]\n" \
  "ds_read_b64_tr_b16 v[118:119], %[wa1] offset:58368\n" \
  "v_mfma_f32_16x16x32_bf16 v[20:23], v[100:103], v[68:71], v[20:23]\n" \
  "ds_read_b128 v[84:87], %[xa1] offset:51200\n" \
  "v_mfma_f32_16x16x32_bf16 v[24:27], v[104:107], v[68:71], v[24:27]\n" \
  "ds_read_b64_tr_b16 v[120:121], %[wa2] offset:57344\n" \
  "v_mfma_f32_16x16x32_bf16 v[28:31], v[108:111], v[68:71], v[28:31]\n" \
  "ds_read_b64_tr_b16 v[122:123], %[wa2] offset:58368\n" \
  "v_mfma_f32_16x16x32_bf16 v[32:35], v[96:99], v[72:75], v[32:35]\n" \
  "ds_read_b128 v[88:91], %[xa1] offset:53248\n" \
  "v_mfma_f32_16x16x32_bf16 v[36:39], v[100:103], v[72:75], v[36:39]\n" \
  "ds_read_b64_tr_b16 v[124:125], %[wa3] offset:57344\n" \
  "v_mfma_f32_16x16x32_bf16 v[40:43], v[104:107], v[72:75], v[40:43]\n" \
  "ds_read_b64_tr_b16 v[126:127], %[wa3] offset:58368\n" \
  "v_mfma_f32_16x16x32_bf16 v[44:47], v[108:111], v[72:75], v[44:47]\n" \
  "ds_read_b128 v[92:95], %[xa1] offset:55296\n" \
  "v_mfma_f32_16x16x32_bf16 v[48:51], v[96:99], v[76:79], v[48:51]\n" \
  "v_mfma_f32_16x16x32_bf16 v[52:55], v[100:103], v[76:79], v[52:55]\n" \
  "v_mfma_f32_16x16x32_bf16 v[56:59], v[104:107], v[76:79], v[56:59]\n" \
  "v_mfma_f32_16x16x32_bf16 v[60:63], v[108:111], v[76:79], v[60:63]\n" \
  "s_waitcnt lgkmcnt(0)\n" \
  "s_waitcnt vmcnt(6)\n" \
  "s_barrier\n" \
  "v_mfma_f32_16x16x32_bf16 v[0:3], v[112:115], v[80:83], v[0:3]\n" \
  "ds_read_b64_tr_b16 v[96:97], v130 offset:0\n" \
  "v_mfma_f32_16x16x32_bf16 v[4:7], v[116:119], v[80:83], v[4:7]\n" \
  "ds_read_b64_tr_b16 v[98:99], v130 offset:1024\n" \
  "v_mfma_f32_16x16x32_bf16 v[8:11], v[120:123], v[80:83], v[8:11]\n" \
  "ds_read_b128 v[64:67], v128 offset:0\n" \
  "v_mfma_f32_16x16x32_bf16 v[12:15], v[124:127], v[80:83], v[12:15]\n" \
  "ds_read_b64_tr_b16 v[100:101], v131 offset:0\n" \
  "v_mfma_f32_16x16x32_bf16 v[16:19], v[112:115], v[84:87], v[16:19]\n" \
  "ds_read_b64_tr_b16 v[102:103], v131 offset:1024\n" \
  "s_add_u32 m0, %[xd], 49152\n" \
  "s_nop 0\n" \
  "global_load_lds_dwordx4 %[xs0], s[40:41]\n" \
  "v_mfma_f32_16x16x32_bf16 v[20:23], v[116:119], v[84:87], v[20:23]\n" \
  "ds_read_b128 v[68:71], v128 offset:2048\n" \
  "v_mfma_f32_16x16x32_bf16 v[24:27], v[120:123], v[84:87], v[24:27]\n" \
  "ds_read_b64_tr_b16 v[104:105], v132 offset:0\n" \
  "s_add_u32 m0, %[xd], 50176\n" \
  "s_nop 0\n" \
  "global_load_lds_dwordx4 %[xs1], s[40:41]\n" \
  "v_mfma_f32_16x16x32_bf16 v[28:31], v[124:127], v[84:87], v[28:31]\n" \
  "ds_read_b64_tr_b16 v[106:107], v132 offset:1024\n" \
  "v_mfma_f32_16x16x32_bf16 v[32:35], v[112:115], v[88:91], v[32:35]\n" \
  "ds_read_b128 v[72:75], v128 offset:4096\n" \
  "s_add_u32 m0, %[xd], 51200\n" \
  "s_nop 0\n" \
  "global_load_lds_dwordx4 %[xs2], s[40:41]\n" \
  "v_mfma_f32_16x16x32_bf16 v[36:39], v[116:119], v[88:91], v[36:39]\n" \
  "ds_read_b64_tr_b16 v[108:109], v133 offset:0\n" \
  "v_mfma_f32_16x16x32_bf16 v[40:43], v[120:123], v[88:91], v[40:43]\n" \
  "ds_read_b64_tr_b16 v[110:111], v133 offset:1024\n" \
  "s_add_u32 m0, %[xd], 52224\n" \
  "s_nop 0\n" \
  "global_load_lds_dwordx4 %[xs3], s[40:41]\n" \
  "v_mfma_f32_16x16x32_bf16 v[44:47], v[124:127], v[88:91], v[44:47]\n" \
  "ds_read_b128 v[76:79], v128 offset:6144\n" \
  "v_mfma_f32_16x16x32_bf16 v[48:51], v[112:115], v[92:95], v[48:51]\n" \
  "s_add_u32 m0, %[wd], 49152\n" \
  "s_nop 0\n" \
  "global_load_lds_dwordx4 %[ws0], s[42:43]\n" \
  "v_mfma_f32_16x16x32_bf16 v[52:55], v[116:119], v[92:95], v[52:55]\n" \
  "v_mfma_f32_16x16x32_bf16 v[56:59], v[120:123], v[92:95], v[56:59]\n" \
  "s_add_u32 m0, %[wd], 50176\n" \
  "s_nop 0\n" \
  "global_load_lds_dwordx4 %[ws1], s[42:43]\n" \
  "s_add_u32 s48, s48, 128\n" \
  "s_cmp_eq_u32 s48, 1280\n" \
  "s_cselect_b32 s48, 0, s48\n" \
  "s_add_u32 s40, s44, s48\n" \
  "s_addc_u32 s41, s45, 0\n" \
  "s_mul_i32 s49, s48, 1920\n" \
  "s_add_u32 s42, s46, s49\n" \
  "s_addc_u32 s43, s47, 0\n" \
  "v_mfma_f32_16x16x32_bf16 v[60:63], v[124:127], v[92:95], v[60:63]\n" \
  "s_waitcnt lgkmcnt(0)\n" \
  "v_mfma_f32_16x16x32_bf16 v[0:3], v[96:99], v[64:67], v[0:3]\n" \
  "ds_read_b64_tr_b16 v[112:113], v130 offset:8192\n" \
  "v_mfma_f32_16x16x32_bf16 v[4:7], v[100:103], v[64:67], v[4:7]\n" \
  "ds_read_b64_tr_b16 v[114:115], v130 offset:9216\n" \
  "v_mfma_f32_16x16x32_bf16 v[8:11], v[104:107], v[64:67], v[8:11]\n" \
  "ds_read_b128 v[80:83], v129 offset:0\n" \
  "v_mfma_f32_16x16x32_bf16 v[12:15], v[108:111], v[64:67], v[12:15]\n" \
  "ds_read_b64_tr_b16 v[116:117], v131 offset:8192\n" \
  "v_mfma_f32_16x16x32_bf16 v[16:19], v[96:99], v[68:71], v[16:19]\n" \
  "ds_read_b64_tr_b16 v[118:119], v131 offset:9216\n" \
  "v_mfma_f32_16x16x32_bf16 v[20:23], v[100:103], v[68:71], v[20:23]\n" \
  "ds_read_b128 v[84:87], v129 offset:2048\n" \
  "v_mfma_f32_16x16x32_bf16 v[24:27], v[104:107], v[68:71], v[24:27]\n" \
  "ds_read_b64_tr_b16 v[120:121], v132 offset:8192\n" \
  "v_mfma_f32_16x16x32_bf16 v[28:31], v[108:111], v[68:71], v[28:31]\n" \
  "ds_read_b64_tr_b16 v[122:123], v132 offset:9216\n" \
  "v_mfma_f32_16x16x32_bf16 v[32:35], v[96:99], v[72:75], v[32:35]\n" \
  "ds_read_b128 v[88:91], v129 offset:4096\n" \
  "v_mfma_f32_16x16x32_bf16 v[36:39], v[100:103], v[72:75], v[36:39]\n" \
  "ds_read_b64_tr_b16 v[124:125], v133 offset:8192\n" \
  "v_mfma_f32_16x16x32_bf16 v[40:43], v[104:107], v[72:75], v[40:43]\n" \
  "ds_read_b64_tr_b16 v[126:127], v133 offset:9216\n" \
  "v_mfma_f32_16x16x32_bf16 v[44:47], v[108:111], v[72:75], v[44:47]\n" \
  "ds_read_b128 v[92:95], v129 offset:6144\n" \
  "v_mfma_f32_16x16x32_bf16 v[48:51], v[96:99], v[76:79], v[48:51]\n" \
  "v_mfma_f32_16x16x32_bf16 v[52:55], v[100:103], v[76:79], v[52:55]\n" \
  "v_mfma_f32_16x16x32_bf16 v[56:59], v[104:107], v[76:79], v[56:59]\n" \
  "v_mfma_f32_16x16x32_bf16 v[60:63], v[108:111], v[76:79], v[60:63]\n" \
  "s_waitcnt lgkmcnt(0)\n" \
  "s_waitcnt vmcnt(6)\n" \
  "s_barrier\n" \
  "v_mfma_f32_16x16x32_bf16 v[0:3], v[112:115], v[80:83], v[0:3]\n" \
  "ds_read_b64_tr_b16 v[96:97], %[wa0] offset:0\n" \
  "v_mfma_f32_16x16x32_bf16 v[4:7], v[116:119], v[80:83], v[4:7]\n" \
  "ds_read_b64_tr_b16 v[98:99], %[wa0] offset:1024\n" \
  "v_mfma_f32_16x16x32_bf16 v[8:11], v[120:123], v[80:83], v[8:11]\n" \
  "ds_read_b128 v[64:67], %[xa0] offset:0\n" \
  "v_mfma_f32_16x16x32_bf16 v[12:15], v[124:127], v[80:83], v[12:15]\n" \
  "ds_read_b64_tr_b16 v[100:101], %[wa1] offset:0\n" \
  "v_mfma_f32_16x16x32_bf16 v[16:19], v[112:115], v[84:87], v[16:19]\n" \
  "ds_read_b64_tr_b16 v[102:103], %[wa1] offset:1024\n" \
  "s_add_u32 m0, %[xd], 98304\n" \
  "s_nop 0\n" \
  "global_load_lds_dwordx4 %[xs0], s[40:41]\n" \
  "v_mfma_f32_16x16x32_bf16 v[20:23], v[116:119], v[84:87], v[20:23]\n" \
  "ds_read_b128 v[68:71], %[xa0] offset:2048\n" \
  "v_mfma_f32_16x16x32_bf16 v[24:27], v[120:123], v[84:87], v[24:27]\n" \
  "ds_read_b64_tr_b16 v[104:105], %[wa2] offset:0\n" \
  "s_add_u32 m0, %[xd], 99328\n" \
  "s_nop 0\n" \
  "global_load_lds_dwordx4 %[xs1], s[40:41]\n" \
  "v_mfma_f32_16x16x32_bf16 v[28:31], v[124:127], v[84:87], v[28:31]\n" \
  "ds_read_b64_tr_b16 v[106:107], %[wa2] offset:1024\n" \
  "v_mfma_f32_16x16x32_bf16 v[32:35], v[112:115], v[88:91], v[32:35]\n" \
  "ds_read_b128 v[72:75], %[xa0] offset:4096\n" \
  "s_add_u32 m0, %[xd], 100352\n" \
  "s_nop 0\n" \
  "global_load_lds_dwordx4 %[xs2], s[40:41]\n" \
  "v_mfma_f32_16x16x32_bf16 v[36:39], v[116:119], v[88:91], v[36:39]\n" \
  "ds_read_b64_tr_b16 v[108:109], %[wa3] offset:0\n" \
  "v_mfma_f32_16x16x32_bf16 v[40:43], v[120:123], v[88:91], v[40:43]\n" \
  "ds_read_b64_tr_b16 v[110:111], %[wa3] offset:1024\n" \
  "s_add_u32 m0, %[xd], 101376\n" \
  "s_nop 0\n" \
  "global_load_lds_dwordx4 %[xs3], s[40:41]\n" \
  "v_mfma_f32_16x16x32_bf16 v[44:47], v[124:127], v[88:91], v[44:47]\n" \
  "ds_read_b128 v[76:79], %[xa0] offset:6144\n" \
  "v_mfma_f32_16x16x32_bf16 v[48:51], v[112:115], v[92:95], v[48:51]\n" \
  "s_add_u32 m0, %[wd], 98304\n" \
  "s_nop 0\n" \
  "global_load_lds_dwordx4 %[ws0], s[42:43]\n" \
  "v_mfma_f32_16x16x32_bf16 v[52:55], v[116:119], v[92:95], v[52:55]\n" \
  "v_mfma_f32_16x16x32_bf16 v[56:59], v[120:123], v[92:95], v[56:59]\n" \
  "s_add_u32 m0, %[wd], 99328\n" \
  "s_nop 0\n" \
  "global_load_lds_dwordx4 %[ws1], s[42:43]\n" \
  "s_add_u32 s48, s48, 128\n" \
  "s_cmp_eq_u32 s48, 1280\n" \
  "s_cselect_b32 s48, 0, s48\n" \
  "s_add_u32 s40, s44, s48\n" \
  "s_addc_u32 s41, s45, 0\n" \
  "s_mul_i32 s49, s48, 1920\n" \
  "s_add_u32 s42, s46, s49\n" \
  "s_addc_u32 s43, s47, 0\n" \
  "v_mfma_f32_16x16x32_bf16 v[60:63], v[124:127], v[92:95], v[60:63]\n" \
  "s_waitcnt lgkmcnt(0)\n" \
  "v_mfma_f32_16x16x32_bf16 v[0:3], v[96:99], v[64:67], v[0:3]\n" \
  "ds_read_b64_tr_b16 v[112:113], %[wa0] offset:8192\n" \
  "v_mfma_f32_16x16x32_bf16 v[4:7], v[100:103], v[64:67], v[4:7]\n" \
  "ds_read_b64_tr_b16 v[114:115], %[wa0] offset:9216\n" \
  "v_mfma_f32_16x16x32_bf16 v[8:11], v[104:107], v[64:67], v[8:11]\n" \
  "ds_read_b128 v[80:83], %[xa1] offset:0\n" \
  "v_mfma_f32_16x16x32_bf16 v[12:15], v[108:111], v[64:67], v[12:15]\n" \
  "ds_read_b64_tr_b16 v[116:117], %[wa1] offset:8192\n" \
  "v_mfma_f32_16x16x32_bf16 v[16:19], v[96:99], v[68:71], v[16:19]\n" \
  "ds_read_b64_tr_b16 v[118:119], %[wa1] offset:9216\n" \
  "v_mfma_f32_16x16x32_bf16 v[20:23], v[100:103], v[68:71], v[20:23]\n" \
  "ds_read_b128 v[84:87], %[xa1] offset:2048\n" \
  "v_mfma_f32_16x16x32_bf16 v[24:27], v[104:107], v[68:71], v[24:27]\n" \
  "ds_read_b64_tr_b16 v[120:121], %[wa2] offset:8192\n" \
  "v_mfma_f32_16x16x32_bf16 v[28:31], v[108:111], v[68:71], v[28:31]\n" \
  "ds_read_b64_tr_b16 v[122:123], %[wa2] offset:9216\n" \
  "v_mfma_f32_16x16x32_bf16 v[32:35], v[96:99], v[72:75], v[32:35]\n" \
  "ds_read_b128 v[88:91], %[xa1] offset:4096\n" \
  "v_mfma_f32_16x16x32_bf16 v[36:39], v[100:103], v[72:75], v[36:39]\n" \
  "ds_read_b64_tr_b16 v[124:125], %[wa3] offset:8192\n" \
  "v_mfma_f32_16x16x32_bf16 v[40:43], v[104:107], v[72:75], v[40:43]\n" \
  "ds_read_b64_tr_b16 v[126:127], %[wa3] offset:9216\n" \
  "v_mfma_f32_16x16x32_bf16 v[44:47], v[108:111], v[72:75], v[44:47]\n" \
  "ds_read_b128 v[92:95], %[xa1] offset:6144\n" \
  "v_mfma_f32_16x16x32_bf16 v[48:51], v[96:99], v[76:79], v[48:51]\n" \
  "v_mfma_f32_16x16x32_bf16 v[52:55], v[100:103], v[76:79], v[52:55]\n" \
  "v_mfma_f32_16x16x32_bf16 v[56:59], v[104:107], v[76:79], v[56:59]\n" \
  "v_mfma_f32_16x16x32_bf16 v[60:63], v[108:111], v[76:79], v[60:63]\n" \
  "s_waitcnt lgkmcnt(0)\n" \
  "s_waitcnt vmcnt(6)\n" \
  "s_barrier\n" \
  "v_mfma_f32_16x16x32_bf16 v[0:3], v[112:115], v[80:83], v[0:3]\n" \
  "ds_read_b64_tr_b16 v[96:97], %[wa0] offset:49152\n" \
  "v_mfma_f32_16x16x32_bf16 v[4:7], v[116:119], v[80:83], v[4:7]\n" \
  "ds_read_b64_tr_b16 v[98:99], %[wa0] offset:50176\n" \
  "v_mfma_f32_16x16x32_bf16 v[8:11], v[120:123], v[80:83], v[8:11]\n" \
  "ds_read_b128 v[64:67], %[xa0] offset:49152\n" \
  "v_mfma_f32_16x16x32_bf16 v[12:15], v[124:127], v[80:83], v[12:15]\n" \
  "ds_read_b64_tr_b16 v[100:101], %[wa1] offset:49152\n" \
  "v_mfma_f32_16x16x32_bf16 v[16:19], v[112:115], v[84:87], v[16:19]\n" \
  "ds_read_b64_tr_b16 v[102:103], %[wa1] offset:50176\n" \
  "s_add_u32 m0, %[xd], 0\n" \
  "s_nop 0\n" \
  "global_load_lds_dwordx4 %[xs0], s[40:41]\n" \
  "v_mfma_f32_16x16x32_bf16 v[20:23], v[116:119], v[84:87], v[20:23]\n" \
  "ds_read_b128 v[68:71], %[xa0] offset:51200\n" \
  "v_mfma_f32_16x16x32_bf16 v[24:27], v[120:123], v[84:87], v[24:27]\n" \
  "ds_read_b64_tr_b16 v[104:105], %[wa2] offset:49152\n" \
  "s_add_u32 m0, %[xd], 1024\n" \
  "s_nop 0\n" \
  "global_load_lds_dwordx4 %[xs1], s[40:41]\n" \
  "v_mfma_f32_16x16x32_bf16 v[28:31], v[124:127], v[84:87], v[28:31]\n" \
  "ds_read_b64_tr_b16 v[106:107], %[wa2] offset:50176\n" \
  "v_mfma_f32_16x16x32_bf16 v[32:35], v[112:115], v[88:91], v[32:35]\n" \
  "ds_read_b128 v[72:75], %[xa0] offset:53248\n" \
  "s_add_u32 m0, %[xd], 2048\n" \
  "s_nop 0\n" \
  "global_load_lds_dwordx4 %[xs2], s[40:41]\n" \
  "v_mfma_f32_16x16x32_bf16 v[36:39], v[116:119], v[88:91], v[36:39]\n" \
  "ds_read_b64_tr_b16 v[108:109], %[wa3] offset:49152\n" \
  "v_mfma_f32_16x16x32_bf16 v[40:43], v[120:123], v[88:91], v[40:43]\n" \
  "ds_read_b64_tr_b16 v[110:111], %[wa3] offset:50176\n" \
  "s_add_u32 m0, %[xd], 3072\n" \
  "s_nop 0\n" \
  "global_load_lds_dwordx4 %[xs3], s[40:41]\n" \
  "v_mfma_f32_16x16x32_bf16 v[44:47], v[124:127], v[88:91], v[44:47]\n" \
  "ds_read_b128 v[76:79], %[xa0] offset:55296\n" \
  "v_mfma_f32_16x16x32_bf16 v[48:51], v[112:115], v[92:95], v[48:51]\n" \
  "s_add_u32 m0, %[wd], 0\n" \
  "s_nop 0\n" \
  "global_load_lds_dwordx4 %[ws0], s[42:43]\n" \
  "v_mfma_f32_16x16x32_bf16 v[52:55], v[116:119], v[92:95], v[52:55]\n" \
  "v_mfma_f32_16x16x32_bf16 v[56:59], v[120:123], v[92:95], v[56:59]\n" \
  "s_add_u32 m0, %[wd], 1024\n" \
  "s_nop 0\n" \
  "global_load_lds_dwordx4 %[ws1], s[42:43]\n" \
  "s_add_u32 s48, s48, 128\n" \
  "s_cmp_eq_u32 s48, 1280\n" \
  "s_cselect_b32 s48, 0, s48\n" \
  "s_add_u32 s40, s44, s48\n" \
  "s_addc_u32 s41, s45, 0\n" \
  "s_mul_i32 s49, s48, 1920\n" \
  "s_add_u32 s42, s46, s49\n" \
  "s_addc_u32 s43, s47, 0\n" \
  "v_mfma_f32_16x16x32_bf16 v[60:63], v[124:127], v[92:95], v[60:63]\n" \
  "s_waitcnt lgkmcnt(0)\n" \
  "global_load_dword %[rv], %[ro], %[wsb] sc1\n" \
  "v_mfma_f32_16x16x32_bf16 v[0:3], v[96:99], v[64:67], v[0:3]\n" \
  "ds_read_b64_tr_b16 v[112:113], %[wa0] offset:57344\n" \
  "v_mfma_f32_16x16x32_bf16 v[4:7], v[100:103], v[64:67], v[4:7]\n" \
  "ds_read_b64_tr_b16 v[114:115], %[wa0] offset:58368\n" \
  "v_mfma_f32_16x16x32_bf16 v[8:11], v[104:107], v[64:67], v[8:11]\n" \
  "ds_read_b128 v[80:83], %[xa1] offset:49152\n" \
  "v_mfma_f32_16x16x32_bf16 v[12:15], v[108:111], v[64:67], v[12:15]\n" \
  "ds_read_b64_tr_b16 v[116:117], %[wa1] offset:57344\n" \
  "v_mfma_f32_16x16x32_bf16 v[16:19], v[96:99], v[68:71], v[16:19]\n" \
  "ds_read_b64_tr_b16 v[118:119], %[wa1] offset:58368\n" \
  "v_mfma_f32_16x16x32_bf16 v[20:23], v[100:103], v[68:71], v[20:23]\n" \
  "ds_read_b128 v[84:87], %[xa1] offset:51200\n" \
  "v_mfma_f32_16x16x32_bf16 v[24:27], v[104:107], v[68:71], v[24:27]\n" \
  "ds_read_b64_tr_b16 v[120:121], %[wa2] offset:57344\n" \
  "v_mfma_f32_16x16x32_bf16 v[28:31], v[108:111], v[68:71], v[28:31]\n" \
  "ds_read_b64_tr_b16 v[122:123], %[wa2] offset:58368\n" \
  "v_mfma_f32_16x16x32_bf16 v[32:35], v[96:99], v[72:75], v[32:35]\n" \
  "ds_read_b128 v[88:91], %[xa1] offset:53248\n" \
  "v_mfma_f32_16x16x32_bf16 v[36:39], v[100:103], v[72:75], v[36:39]\n" \
  "ds_read_b64_tr_b16 v[124:125], %[wa3] offset:57344\n" \
  "v_mfma_f32_16x16x32_bf16 v[40:43], v[104:107], v[72:75], v[40:43]\n" \
  "ds_read_b64_tr_b16 v[126:127], %[wa3] offset:58368\n" \
  "v_mfma_f32_16x16x32_bf16 v[44:47], v[108:111], v[72:75], v[44:47]\n" \
  "ds_read_b128 v[92:95], %[xa1] offset:55296\n" \
  "v_mfma_f32_16x16x32_bf16 v[48:51], v[96:99], v[76:79], v[48:51]\n" \
  "v_mfma_f32_16x16x32_bf16 v[52:55], v[100:103], v[76:79], v[52:55]\n" \
  "v_mfma_f32_16x16x32_bf16 v[56:59], v[104:107], v[76:79], v[56:59]\n" \
  "v_mfma_f32_16x16x32_bf16 v[60:63], v[108:111], v[76:79], v[60:63]\n" \
  "s_waitcnt lgkmcnt(0)\n" \
  "s_waitcnt vmcnt(7)\n" \
  "s_barrier\n" \
  "v_mfma_f32_16x16x32_bf16 v[0:3], v[112:115], v[80:83], v[0:3]\n" \
  "ds_read_b64_tr_b16 v[96:97], v130 offset:0\n" \
  "v_mfma_f32_16x16x32_bf16 v[4:7], v[116:119], v[80:83], v[4:7]\n" \
  "ds_read_b64_tr_b16 v[98:99], v130 offset:1024\n" \
  "v_mfma_f32_16x16x32_bf16 v[8:11], v[120:123], v[80:83], v[8:11]\n" \
  "ds_read_b128 v[64:67], v128 offset:0\n" \
  "v_mfma_f32_16x16x32_bf16 v[12:15], v[124:127], v[80:83], v[12:15]\n" \
  "ds_read_b64_tr_b16 v[100:101], v131 offset:0\n" \
  "v_mfma_f32_16x16x32_bf16 v[16:19], v[112:115], v[84:87], v[16:19]\n" \
  "ds_read_b64_tr_b16 v[102:103], v131 offset:1024\n" \
  "v_mfma_f32_16x16x32_bf16 v[20:23], v[116:119], v[84:87], v[20:23]\n" \
  "ds_read_b128 v[68:71], v128 offset:2048\n" \
  "v_mfma_f32_16x16x32_bf16 v[24:27], v[120:123], v[84:87], v[24:27]\n" \
  "ds_read_b64_tr_b16 v[104:105], v132 offset:0\n" \
  "v_mfma_f32_16x16x32_bf16 v[28:31], v[124:127], v[84:87], v[28:31]\n" \
  "ds_read_b64_tr_b16 v[106:107], v132 offset:1024\n" \
  "v_mfma_f32_16x16x32_bf16 v[32:35], v[112:115], v[88:91], v[32:35]\n" \
  "ds_read_b128 v[72:75], v128 offset:4096\n" \
  "v_mfma_f32_16x16x32_bf16 v[36:39], v[116:119], v[88:91], v[36:39]\n" \
  "ds_read_b64_tr_b16 v[108:109], v133 offset:0\n" \
  "v_mfma_f32_16x16x32_bf16 v[40:43], v[120:123], v[88:91], v[40:43]\n" \
  "ds_read_b64_tr_b16 v[110:111], v133 offset:1024\n" \
  "v_mfma_f32_16x16x32_bf16 v[44:47], v[124:127], v[88:91], v[44:47]\n" \
  "ds_read_b128 v[76:79], v128 offset:6144\n" \
  "v_mfma_f32_16x16x32_bf16 v[48:51], v[112:115], v[92:95], v[48:51]\n" \
  "v_mfma_f32_16x16x32_bf16 v[52:55], v[116:119], v[92:95], v[52:55]\n" \
  "v_mfma_f32_16x16x32_bf16 v[56:59], v[120:123], v[92:95], v[56:59]\n" \
  "v_mfma_f32_16x16x32_bf16 v[60:63], v[124:127], v[92:95], v[60:63]\n" \
  "s_waitcnt lgkmcnt(0)\n" \
  "v_mfma_f32_16x16x32_bf16 v[0:3], v[96:99], v[64:67], v[0:3]\n" \
  "ds_read_b64_tr_b16 v[112:113], v130 offset:8192\n" \
  "v_mfma_f32_16x16x32_bf16 v[4:7], v[100:103], v[64:67], v[4:7]\n" \
  "ds_read_b64_tr_b16 v[114:115], v130 offset:9216\n" \
  "v_mfma_f32_16x16x32_bf16 v[8:11], v[104:107], v[64:67], v[8:11]\n" \
  "ds_read_b128 v[80:83], v129 offset:0\n" \
  "v_mfma_f32_16x16x32_bf16 v[12:15], v[108:111], v[64:67], v[12:15]\n" \
  "ds_read_b64_tr_b16 v[116:117], v131 offset:8192\n" \
  "v_mfma_f32_16x16x32_bf16 v[16:19], v[96:99], v[68:71], v[16:19]\n" \
  "ds_read_b64_tr_b16 v[118:119], v131 offset:9216\n" \
  "v_mfma_f32_16x16x32_bf16 v[20:23], v[100:103], v[68:71], v[20:23]\n" \
  "ds_read_b128 v[84:87], v129 offset:2048\n" \
  "v_mfma_f32_16x16x32_bf16 v[24:27], v[104:107], v[68:71], v[24:27]\n" \
  "ds_read_b64_tr_b16 v[120:121], v132 offset:8192\n" \
  "v_mfma_f32_16x16x32_bf16 v[28:31], v[108:111], v[68:71], v[28:31]\n" \
  "ds_read_b64_tr_b16 v[122:123], v132 offset:9216\n" \
  "v_mfma_f32_16x16x32_bf16 v[32:35], v[96:99], v[72:75], v[32:35]\n" \
  "ds_read_b128 v[88:91], v129 offset:4096\n" \
  "v_mfma_f32_16x16x32_bf16 v[36:39], v[100:103], v[72:75], v[36:39]\n" \
  "ds_read_b64_tr_b16 v[124:125], v133 offset:8192\n" \
  "v_mfma_f32_16x16x32_bf16 v[40:43], v[104:107], v[72:75], v[40:43]\n" \
  "ds_read_b64_tr_b16 v[126:127], v133 offset:9216\n" \
  "v_mfma_f32_16x16x32_bf16 v[44:47], v[108:111], v[72:75], v[44:47]\n" \
  "ds_read_b128 v[92:95], v129 offset:6144\n" \
  "v_mfma_f32_16x16x32_bf16 v[48:51], v[96:99], v[76:79], v[48:51]\n" \
  "v_mfma_f32_16x16x32_bf16 v[52:55], v[100:103], v[76:79], v[52:55]\n" \
  "v_mfma_f32_16x16x32_bf16 v[56:59], v[104:107], v[76:79], v[56:59]\n" \
  "v_mfma_f32_16x16x32_bf16 v[60:63], v[108:111], v[76:79], v[60:63]\n" \
  "s_waitcnt lgkmcnt(0)\n" \
  "s_waitcnt vmcnt(0)\n" \
  "s_barrier\n" \
  "s_cmp_lg_u32 %[w], 7\n" \
  "s_cbranch_scc1 L_noack_%=\n" \
  "v_readfirstlane_b32 s49, %[rv]\n" \
  "s_cmp_lg_u32 s49, 1\n" \
  "s_cbranch_scc1 L_noack_%=\n" \
  "s_mov_b64 exec, 1\n" \
  "global_store_dword %[ro], %[rv], %[wsb] offset:4 sc1\n" \
  "s_mov_b64 exec, -1\n" \
  "L_noack_%=:\n" \
  "v_mfma_f32_16x16x32_bf16 v[0:3], v[112:115], v[80:83], v[0:3]\n" \
  "ds_read_b64_tr_b16 v[96:97], %[wa0] offset:0\n" \
  "v_mfma_f32_16x16x32_bf16 v[4:7], v[116:119], v[80:83], v[4:7]\n" \
  "ds_read_b64_tr_b16 v[98:99], %[wa0] offset:1024\n" \
  "v_mfma_f32_16x16x32_bf16 v[8:11], v[120:123], v[80:83], v[8:11]\n" \
  "ds_read_b128 v[64:67], %[xa0] offset:0\n" \
  "v_mfma_f32_16x16x32_bf16 v[12:15], v[124:127], v[80:83], v[12:15]\n" \
  "ds_read_b64_tr_b16 v[100:101], %[wa1] offset:0\n" \
  "v_mfma_f32_16x16x32_bf16 v[16:19], v[112:115], v[84:87], v[16:19]\n" \
  "ds_read_b64_tr_b16 v[102:103], %[wa1] offset:1024\n" \
  "v_mfma_f32_16x16x32_bf16 v[20:23], v[116:119], v[84:87], v[20:23]\n" \
  "ds_read_b128 v[68:71], %[xa0] offset:2048\n" \
  "v_mfma_f32_16x16x32_bf16 v[24:27], v[120:123], v[84:87], v[24:27]\n" \
  "ds_read_b64_tr_b16 v[104:105], %[wa2] offset:0\n" \
  "v_mfma_f32_16x16x32_bf16 v[28:31], v[124:127], v[84:87], v[28:31]\n" \
  "ds_read_b64_tr_b16 v[106:107], %[wa2] offset:1024\n" \
  "v_mfma_f32_16x16x32_bf16 v[32:35], v[112:115], v[88:91], v[32:35]\n" \
  "ds_read_b128 v[72:75], %[xa0] offset:4096\n" \
  "v_mfma_f32_16x16x32_bf16 v[36:39], v[116:119], v[88:91], v[36:39]\n" \
  "ds_read_b64_tr_b16 v[108:109], %[wa3] offset:0\n" \
  "v_mfma_f32_16x16x32_bf16 v[40:43], v[120:123], v[88:91], v[40:43]\n" \
  "ds_read_b64_tr_b16 v[110:111], %[wa3] offset:1024\n" \
  "v_mfma_f32_16x16x32_bf16 v[44:47], v[124:127], v[88:91], v[44:47]\n" \
  "ds_read_b128 v[76:79], %[xa0] offset:6144\n" \
  "v_mfma_f32_16x16x32_bf16 v[48:51], v[112:115], v[92:95], v[48:51]\n" \
  "v_mfma_f32_16x16x32_bf16 v[52:55], v[116:119], v[92:95], v[52:55]\n" \
  "v_mfma_f32_16x16x32_bf16 v[56:59], v[120:123], v[92:95], v[56:59]\n" \
  "v_mfma_f32_16x16x32_bf16 v[60:63], v[124:127], v[92:95], v[60:63]\n" \
  "s_waitcnt lgkmcnt(0)\n" \
  "v_mov_b32 v128, %[qs]\n" \
  "v_mov_b32 v129, %[qs]\n" \
  "v_mfma_f32_16x16x32_bf16 v[0:3], v[96:99], v[64:67], v[0:3]\n" \
  "ds_read_b64_tr_b16 v[112:113], %[wa0] offset:8192\n" \
  "ds_read_b64_tr_b16 v[114:115], %[wa0] offset:9216\n" \
  "v_mfma_f32_16x16x32_bf16 v[16:19], v[96:99], v[68:71], v[16:19]\n" \
  "ds_read_b128 v[80:83], %[xa1] offset:0\n" \
  "ds_read_b64_tr_b16 v[116:117], %[wa1] offset:8192\n" \
  "v_mfma_f32_16x16x32_bf16 v[32:35], v[96:99], v[72:75], v[32:35]\n" \
  "ds_read_b64_tr_b16 v[118:119], %[wa1] offset:9216\n" \
  "ds_read_b128 v[84:87], %[xa1] offset:2048\n" \
  "v_mfma_f32_16x16x32_bf16 v[48:51], v[96:99], v[76:79], v[48:51]\n" \
  "ds_read_b64_tr_b16 v[120:121], %[wa2] offset:8192\n" \
  "ds_read_b64_tr_b16 v[122:123], %[wa2] offset:9216\n" \
  "v_mfma_f32_16x16x32_bf16 v[4:7], v[100:103], v[64:67], v[4:7]\n" \
  "ds_read_b128 v[88:91], %[xa1] offset:4096\n" \
  "ds_read_b64_tr_b16 v[124:125], %[wa3] offset:8192\n" \
  "v_mfma_f32_16x16x32_bf16 v[20:23], v[100:103], v[68:71], v[20:23]\n" \
  "ds_read_b64_tr_b16 v[126:127], %[wa3] offset:9216\n" \
  "ds_read_b128 v[92:95], %[xa1] offset:6144\n" \
  "v_mfma_f32_16x16x32_bf16 v[36:39], v[100:103], v[72:75], v[36:39]\n" \
  "v_mfma_f32_16x16x32_bf16 v[52:55], v[100:103], v[76:79], v[52:55]\n" \
  "s_waitcnt lgkmcnt(0)\n" \
  "v_mfma_f32_16x16x32_bf16 v[0:3], v[112:115], v[80:83], v[0:3]\n" \
  "v_mfma_f32_16x16x32_bf16 v[16:19], v[112:115], v[84:87], v[16:19]\n" \
  "v_mfma_f32_16x16x32_bf16 v[32:35], v[112:115], v[88:91], v[32:35]\n" \
  "v_mfma_f32_16x16x32_bf16 v[48:51], v[112:115], v[92:95], v[48:51]\n" \
  "v_mfma_f32_16x16x32_bf16 v[4:7], v[116:119], v[80:83], v[4:7]\n" \
  "v_mfma_f32_16x16x32_bf16 v[20:23], v[116:119], v[84:87], v[20:23]\n" \
  "s_nop 5\n" \
  "v_pk_mul_f32 v[0:1], v[0:1], v[128:129]\n" \
  "v_pk_mul_f32 v[2:3], v[2:3], v[128:129]\n" \
  "v_pk_mul_f32 v[16:17], v[16:17], v[128:129]\n" \
  "v_pk_mul_f32 v[18:19], v[18:19], v[128:129]\n" \
  "v_pk_mul_f32 v[32:33], v[32:33], v[128:129]\n" \
  "v_pk_mul_f32 v[34:35], v[34:35], v[128:129]\n" \
  "v_mfma_f32_16x16x32_bf16 v[36:39], v[116:119], v[88:91], v[36:39]\n" \
  "v_pk_mul_f32 v[48:49], v[48:49], v[128:129]\n" \
  "v_pk_mul_f32 v[50:51], v[50:51], v[128:129]\n" \
  "v_cvt_pk_bf16_f32 v0, v0, v1\n" \
  "v_cvt_pk_bf16_f32 v1, v2, v3\n" \
  "v_cvt_pk_bf16_f32 v2, v16, v17\n" \
  "v_cvt_pk_bf16_f32 v3, v18, v19\n" \
  "v_mfma_f32_16x16x32_bf16 v[52:55], v[116:119], v[92:95], v[52:55]\n" \
  "v_cvt_pk_bf16_f32 v32, v32, v33\n" \
  "v_cvt_pk_bf16_f32 v33, v34, v35\n" \
  "v_cvt_pk_bf16_f32 v34, v48, v49\n" \
  "v_cvt_pk_bf16_f32 v35, v50, v51\n" \
  "v_permlane16_swap_b32 v0, v2\n" \
  "v_permlane16_swap_b32 v1, v3\n" \
  "v_mfma_f32_16x16x32_bf16 v[8:11], v[104:107], v[64:67], v[8:11]\n" \
  "v_permlane16_swap_b32 v32, v34\n" \
  "v_permlane16_swap_b32 v33, v35\n" \
  "s_mov_b64 exec, %[em0]\n" \
  "global_store_dwordx4 %[ea0], v[0:3], %[wsb] sc0 sc1\n" \
  "global_store_dwordx4 %[ea0], v[32:35], %[wsb] offset:1024 sc0 sc1\n" \
  "s_not_b64 exec, exec\n" \
  "s_cbranch_execz L_nt0_%=\n" \
  "global_store_dwordx2 %[ea0], v[0:1], %[wsb] sc0 sc1\n" \
  "global_store_dwordx2 %[ea0], v[2:3], %[wsb2] sc0 sc1\n" \
  "global_store_dwordx2 %[ea0], v[32:33], %[wsb] offset:256 sc0 sc1\n" \
  "global_store_dwordx2 %[ea0], v[34:35], %[wsb2] offset:256 sc0 sc1\n" \
  "L_nt0_%=:\n" \
  "s_mov_b64 exec, -1\n" \
  "v_mfma_f32_16x16x32_bf16 v[24:27], v[104:107], v[68:71], v[24:27]\n" \
  "v_pk_mul_f32 v[4:5], v[4:5], v[128:129]\n" \
  "v_pk_mul_f32 v[6:7], v[6:7], v[128:129]\n" \
  "v_pk_mul_f32 v[20:21], v[20:21], v[128:129]\n" \
  "v_pk_mul_f32 v[22:23], v[22:23], v[128:129]\n" \
  "v_pk_mul_f32 v[36:37], v[36:37], v[128:129]\n" \
  "v_pk_mul_f32 v[38:39], v[38:39], v[128:129]\n" \
  "v_mfma_f32_16x16x32_bf16 v[40:43], v[104:107], v[72:75], v[40:43]\n" \
  "v_pk_mul_f32 v[52:53], v[52:53], v[128:129]\n" \
  "v_pk_mul_f32 v[54:55], v[54:55], v[128:129]\n" \
  "v_cvt_pk_bf16_f32 v4, v4, v5\n" \
  "v_cvt_pk_bf16_f32 v5, v6, v7\n" \
  "v_cvt_pk_bf16_f32 v6, v20, v21\n" \
  "v_cvt_pk_bf16_f32 v7, v22, v23\n" \
  "v_mfma_f32_16x16x32_bf16 v[56:59], v[104:107], v[76:79], v[56:59]\n" \
  "v_cvt_pk_bf16_f32 v36, v36, v37\n" \
  "v_cvt_pk_bf16_f32 v37, v38, v39\n" \
  "v_cvt_pk_bf16_f32 v38, v52, v53\n" \
  "v_cvt_pk_bf16_f32 v39, v54, v55\n" \
  "v_permlane16_swap_b32 v4, v6\n" \
  "v_permlane16_swap_b32 v5, v7\n" \
  "v_mfma_f32_16x16x32_bf16 v[8:11], v[120:123], v[80:83], v[8:11]\n" \
  "v_permlane16_swap_b32 v36, v38\n" \
  "v_permlane16_swap_b32 v37, v39\n" \
  "s_mov_b64 exec, %[em1]\n" \
  "global_store_dwordx4 %[ea1], v[4:7], %[wsb] sc0 sc1\n" \
  "global_store_dwordx4 %[ea1], v[36:39], %[wsb] offset:1024 sc0 sc1\n" \
  "s_not_b64 exec, exec\n" \
  "s_cbranch_execz L_nt1_%=\n" \
  "global_store_dwordx2 %[ea1], v[4:5], %[wsb] sc0 sc1\n" \
  "global_store_dwordx2 %[ea1], v[6:7], %[wsb2] sc0 sc1\n" \
  "global_store_dwordx2 %[ea1], v[36:37], %[wsb] offset:256 sc0 sc1\n" \
  "global_store_dwordx2 %[ea1], v[38:39], %[wsb2] offset:256 sc0 sc1\n" \
  "L_nt1_%=:\n" \
  "s_mov_b64 exec, -1\n" \
  "v_mfma_f32_16x16x32_bf16 v[24:27], v[120:123], v[84:87], v[24:27]\n" \
  "v_mfma_f32_16x16x32_bf16 v[40:43], v[120:123], v[88:91], v[40:43]\n" \
  "v_mfma_f32_16x16x32_bf16 v[56:59], v[120:123], v[92:95], v[56:59]\n" \
  "v_mfma_f32_16x16x32_bf16 v[12:15], v[108:111], v[64:67], v[12:15]\n" \
  "v_mfma_f32_16x16x32_bf16 v[28:31], v[108:111], v[68:71], v[28:31]\n" \
  "s_nop 5\n" \
  "v_pk_mul_f32 v[8:9], v[8:9], v[128:129]\n" \
  "v_pk_mul_f32 v[10:11], v[10:11], v[128:129]\n" \
  "v_pk_mul_f32 v[24:25], v[24:25], v[128:129]\n" \
  "v_pk_mul_f32 v[26:27], v[26:27], v[128:129]\n" \
  "v_pk_mul_f32 v[40:41], v[40:41], v[128:129]\n" \
  "v_pk_mul_f32 v[42:43], v[42:43], v[128:129]\n" \
  "v_mfma_f32_16x16x32_bf16 v[44:47], v[108:111], v[72:75], v[44:47]\n" \
  "v_pk_mul_f32 v[56:57], v[56:57], v[128:129]\n" \
  "v_pk_mul_f32 v[58:59], v[58:59], v[128:129]\n" \
  "v_cvt_pk_bf16_f32 v8, v8, v9\n" \
  "v_cvt_pk_bf16_f32 v9, v10, v11\n" \
  "v_cvt_pk_bf16_f32 v10, v24, v25\n" \
  "v_cvt_pk_bf16_f32 v11, v26, v27\n" \
  "v_mfma_f32_16x16x32_bf16 v[60:63], v[108:111], v[76:79], v[60:63]\n" \
  "v_cvt_pk_bf16_f32 v40, v40, v41\n" \
  "v_cvt_pk_bf16_f32 v41, v42, v43\n" \
  "v_cvt_pk_bf16_f32 v42, v56, v57\n" \
  "v_cvt_pk_bf16_f32 v43, v58, v59\n" \
  "v_permlane16_swap_b32 v8, v10\n" \
  "v_permlane16_swap_b32 v9, v11\n" \
  "v_mfma_f32_16x16x32_bf16 v[12:15], v[124:127], v[80:83], v[12:15]\n" \
  "v_permlane16_swap_b32 v40, v42\n" \
  "v_permlane16_swap_b32 v41, v43\n" \
  "s_mov_b64 exec, %[em2]\n" \
  "global_store_dwordx4 %[ea2], v[8:11], %[wsb] sc0 sc1\n" \
  "global_store_dwordx4 %[ea2], v[40:43], %[wsb] offset:1024 sc0 sc1\n" \
  "s_not_b64 exec, exec\n" \
  "s_cbranch_execz L_nt2_%=\n" \
  "global_store_dwordx2 %[ea2], v[8:9], %[wsb] sc0 sc1\n" \
  "global_store_dwordx2 %[ea2], v[10:11], %[wsb2] sc0 sc1\n" \
  "global_store_dwordx2 %[ea2], v[40:41], %[wsb] offset:256 sc0 sc1\n" \
  "global_store_dwordx2 %[ea2], v[42:43], %[wsb2] offset:256 sc0 sc1\n" \
  "L_nt2_%=:\n" \
  "s_mov_b64 exec, -1\n" \
  "v_mfma_f32_16x16x32_bf16 v[28:31], v[124:127], v[84:87], v[28:31]\n" \
  "v_mfma_f32_16x16x32_bf16 v[44:47], v[124:127], v[88:91], v[44:47]\n" \
  "v_mfma_f32_16x16x32_bf16 v[60:63], v[124:127], v[92:95], v[60:63]\n" \
  "s_nop 7\n" \
  "v_pk_mul_f32 v[12:13], v[12:13], v[128:129]\n" \
  "v_pk_mul_f32 v[14:15], v[14:15], v[128:129]\n" \
  "v_pk_mul_f32 v[28:29], v[28:29], v[128:129]\n" \
  "v_pk_mul_f32 v[30:31], v[30:31], v[128:129]\n" \
  "v_pk_mul_f32 v[44:45], v[44:45], v[128:129]\n" \
  "v_pk_mul_f32 v[46:47], v[46:47], v[128:129]\n" \
  "v_pk_mul_f32 v[60:61], v[60:61], v[128:129]\n" \
  "v_pk_mul_f32 v[62:63], v[62:63], v[128:129]\n" \
  "v_cvt_pk_bf16_f32 v12, v12, v13\n" \
  "v_cvt_pk_bf16_f32 v13, v14, v15\n" \
  "v_cvt_pk_bf16_f32 v14, v28, v29\n" \
  "v_cvt_pk_bf16_f32 v15, v30, v31\n" \
  "v_cvt_pk_bf16_f32 v44, v44, v45\n" \
  "v_cvt_pk_bf16_f32 v45, v46, v47\n" \
  "v_cvt_pk_bf16_f32 v46, v60, v61\n" \
  "v_cvt_pk_bf16_f32 v47, v62, v63\n" \
  "v_permlane16_swap_b32 v12, v14\n" \
  "v_permlane16_swap_b32 v13, v15\n" \
  "v_permlane16_swap_b32 v44, v46\n" \
  "v_permlane16_swap_b32 v45, v47\n" \
  "s_mov_b64 exec, %[em3]\n" \
  "global_store_dwordx4 %[ea3], v[12:15], %[wsb] sc0 sc1\n" \
  "global_store_dwordx4 %[ea3], v[44:47], %[wsb] offset:1024 sc0 sc1\n" \
  "s_not_b64 exec, exec\n" \
  "s_cbranch_execz L_nt3_%=\n" \
  "global_store_dwordx2 %[ea3], v[12:13], %[wsb] sc0 sc1\n" \
  "global_store_dwordx2 %[ea3], v[14:15], %[wsb2] sc0 sc1\n" \
  "global_store_dwordx2 %[ea3], v[44:45], %[wsb] offset:256 sc0 sc1\n" \
  "global_store_dwordx2 %[ea3], v[46:47], %[wsb2] offset:256 sc0 sc1\n" \
  "L_nt3_%=:\n" \
  "s_mov_b64 exec, -1\n" \
  ""
#define QKV_ASM_N \
  "s_mov_b64 s[44:45], %[xg]\n" \
  "s_mov_b64 s[46:47], %[wg]\n" \
  "s_mov_b32 s48, %[rot3]\n" \
  "s_add_u32 s40, s44, s48\n" \
  "s_addc_u32 s41, s45, 0\n" \
  "s_mul_i32 s49, s48, 1920\n" \
  "s_add_u32 s42, s46, s49\n" \
  "s_addc_u32 s43, s47, 0\n" \
  "s_add_u32 m0, %[xd], 49152\n" \
  "s_nop 0\n" \
  "global_load_lds_dwordx4 %[xs0], s[40:41]\n" \
  "s_add_u32 m0, %[xd], 50176\n" \
  "s_nop 0\n" \
  "global_load_lds_dwordx4 %[xs1], s[40:41]\n" \
  "s_add_u32 m0, %[xd], 51200\n" \
  "s_nop 0\n" \
  "global_load_lds_dwordx4 %[xs2], s[40:41]\n" \
  "s_add_u32 m0, %[xd], 52224\n" \
  "s_nop 0\n" \
  "global_load_lds_dwordx4 %[xs3], s[40:41]\n" \
  "s_add_u32 m0, %[wd], 49152\n" \
  "s_nop 0\n" \
  "global_load_lds_dwordx4 %[ws0], s[42:43]\n" \
  "s_add_u32 m0, %[wd], 50176\n" \
  "s_nop 0\n" \
  "global_load_lds_dwordx4 %[ws1], s[42:43]\n" \
  "s_add_u32 s48, s48, 128\n" \
  "s_cmp_eq_u32 s48, 1280\n" \
  "s_cselect_b32 s48, 0, s48\n" \
  "s_add_u32 s40, s44, s48\n" \
  "s_addc_u32 s41, s45, 0\n" \
  "s_mul_i32 s49, s48, 1920\n" \
  "s_add_u32 s42, s46, s49\n" \
  "s_addc_u32 s43, s47, 0\n" \
  "v_add_u32 v128, 98304, %[xa0]\n" \
  "v_add_u32 v129, 98304, %[xa1]\n" \
  "v_add_u32 v130, 98304, %[wa0]\n" \
  "v_add_u32 v131, 98304, %[wa1]\n" \
  "v_add_u32 v132, 98304, %[wa2]\n" \
  "v_add_u32 v133, 98304, %[wa3]\n" \
  "s_waitcnt vmcnt(6)\n" \
  "s_barrier\n" \
  "ds_read_b64_tr_b16 v[96:97], %[wa0] offset:0\n" \
  "ds_read_b64_tr_b16 v[98:99], %[wa0] offset:1024\n" \
  "ds_read_b128 v[64:67], %[xa0] offset:0\n" \
  "ds_read_b64_tr_b16 v[100:101], %[wa1] offset:0\n" \
  "ds_read_b64_tr_b16 v[102:103], %[wa1] offset:1024\n" \
  "ds_read_b128 v[68:71], %[xa0] offset:2048\n" \
  "ds_read_b64_tr_b16 v[104:105], %[wa2] offset:0\n" \
  "ds_read_b64_tr_b16 v[106:107], %[wa2] offset:1024\n" \
  "ds_read_b128 v[72:75], %[xa0] offset:4096\n" \
  "ds_read_b64_tr_b16 v[108:109], %[wa3] offset:0\n" \
  "ds_read_b64_tr_b16 v[110:111], %[wa3] offset:1024\n" \
  "ds_read_b128 v[76:79], %[xa0] offset:6144\n" \
  "s_waitcnt lgkmcnt(0)\n" \
  "v_mfma_f32_16x16x32_bf16 v[0:3], v[64:67], v[96:99], 0\n" \
  "ds_read_b64_tr_b16 v[112:113], %[wa0] offset:8192\n" \
  "v_mfma_f32_16x16x32_bf16 v[4:7], v[64:67], v[100:103], 0\n" \
  "ds_read_b64_tr_b16 v[114:115], %[wa0] offset:9216\n" \
  "v_mfma_f32_16x16x32_bf16 v[8:11], v[64:67], v[104:107], 0\n" \
  "ds_read_b128 v[80:83], %[xa1] offset:0\n" \
  "v_mfma_f32_16x16x32_bf16 v[12:15], v[64:67], v[108:111], 0\n" \
  "ds_read_b64_tr_b16 v[116:117], %[wa1] offset:8192\n" \
  "v_mfma_f32_16x16x32_bf16 v[16:19], v[68:71], v[96:99], 0\n" \
  "ds_read_b64_tr_b16 v[118:119], %[wa1] offset:9216\n" \
  "s_add_u32 m0, %[xd], 98304\n" \
  "s_nop 0\n" \
  "global_load_lds_dwordx4 %[xs0], s[40:41]\n" \
  "v_mfma_f32_16x16x32_bf16 v[20:23], v[68:71], v[100:103], 0\n" \
  "ds_read_b128 v[84:87], %[xa1] offset:2048\n" \
  "v_mfma_f32_16x16x32_bf16 v[24:27], v[68:71], v[104:107], 0\n" \
  "ds_read_b64_tr_b16 v[120:121], %[wa2] offset:8192\n" \
  "s_add_u32 m0, %[xd], 99328\n" \
  "s_nop 0\n" \
  "global_load_lds_dwordx4 %[xs1], s[40:41]\n" \
  "v_mfma_f32_16x16x32_bf16 v[28:31], v[68:71], v[108:111], 0\n" \
  "ds_read_b64_tr_b16 v[122:123], %[wa2] offset:9216\n" \
  "v_mfma_f32_16x16x32_bf16 v[32:35], v[72:75], v[96:99], 0\n" \
  "ds_read_b128 v[88:91], %[xa1] offset:4096\n" \
  "s_add_u32 m0, %[xd], 100352\n" \
  "s_nop 0\n" \
  "global_load_lds_dwordx4 %[xs2], s[40:41]\n" \
  "v_mfma_f32_16x16x32_bf16 v[36:39], v[72:75], v[100:103], 0\n" \
  "ds_read_b64_tr_b16 v[124:125], %[wa3] offset:8192\n" \
  "v_mfma_f32_16x16x32_bf16 v[40:43], v[72:75], v[104:107], 0\n" \
  "ds_read_b64_tr_b16 v[126:127], %[wa3] offset:9216\n" \
  "s_add_u32 m0, %[xd], 101376\n" \
  "s_nop 0\n" \
  "global_load_lds_dwordx4 %[xs3], s[40:41]\n" \
  "v_mfma_f32_16x16x32_bf16 v[44:47], v[72:75], v[108:111], 0\n" \
  "ds_read_b128 v[92:95], %[xa1] offset:6144\n" \
  "v_mfma_f32_16x16x32_bf16 v[48:51], v[76:79], v[96:99], 0\n" \
  "s_add_u32 m0, %[wd], 98304\n" \
  "s_nop 0\n" \
  "global_load_lds_dwordx4 %[ws0], s[42:43]\n" \
  "v_mfma_f32_16x16x32_bf16 v[52:55], v[76:79], v[100:103], 0\n" \
  "v_mfma_f32_16x16x32_bf16 v[56:59], v[76:79], v[104:107], 0\n" \
  "s_add_u32 m0, %[wd], 99328\n" \
  "s_nop 0\n" \
  "global_load_lds_dwordx4 %[ws1], s[42:43]\n" \
  "s_add_u32 s48, s48, 128\n" \
  "s_cmp_eq_u32 s48, 1280\n" \
  "s_cselect_b32 s48, 0, s48\n" \
  "s_add_u32 s40, s44, s48\n" \
  "s_addc_u32 s41, s45, 0\n" \
  "s_mul_i32 s49, s48, 1920\n" \
  "s_add_u32 s42, s46, s49\n" \
  "s_addc_u32 s43, s47, 0\n" \
  "v_mfma_f32_16x16x32_bf16 v[60:63], v[76:79], v[108:111], 0\n" \
  "s_waitcnt lgkmcnt(0)\n" \
  "s_waitcnt vmcnt(6)\n" \
  "s_barrier\n" \
  "v_mfma_f32_16x16x32_bf16 v[0:3], v[80:83], v[112:115], v[0:3]\n" \
  "ds_read_b64_tr_b16 v[96:97], %[wa0] offset:49152\n" \
  "v_mfma_f32_16x16x32_bf16 v[4:7], v[80:83], v[116:119], v[4:7]\n" \
  "ds_read_b64_tr_b16 v[98:99], %[wa0] offset:50176\n" \
  "v_mfma_f32_16x16x32_bf16 v[8:11], v[80:83], v[120:123], v[8:11]\n" \
  "ds_read_b128 v[64:67], %[xa0] offset:49152\n" \
  "v_mfma_f32_16x16x32_bf16 v[12:15], v[80:83], v[124:127], v[12:15]\n" \
  "ds_read_b64_tr_b16 v[100:101], %[wa1] offset:49152\n" \
  "v_mfma_f32_16x16x32_bf16 v[16:19], v[84:87], v[112:115], v[16:19]\n" \
  "ds_read_b64_tr_b16 v[102:103], %[wa1] offset:50176\n" \
  "s_add_u32 m0, %[xd], 0\n" \
  "s_nop 0\n" \
  "global_load_lds_dwordx4 %[xs0], s[40:41]\n" \
  "v_mfma_f32_16x16x32_bf16 v[20:23], v[84:87], v[116:119], v[20:23]\n" \
  "ds_read_b128 v[68:71], %[xa0] offset:51200\n" \
  "v_mfma_f32_16x16x32_bf16 v[24:27], v[84:87], v[120:123], v[24:27]\n" \
  "ds_read_b64_tr_b16 v[104:105], %[wa2] offset:49152\n" \
  "s_add_u32 m0, %[xd], 1024\n" \
  "s_nop 0\n" \
  "global_load_lds_dwordx4 %[xs1], s[40:41]\n" \
  "v_mfma_f32_16x16x32_bf16 v[28:31], v[84:87], v[124:127], v[28:31]\n" \
  "ds_read_b64_tr_b16 v[106:107], %[wa2] offset:50176\n" \
  "v_mfma_f32_16x16x32_bf16 v[32:35], v[88:91], v[112:115], v[32:35]\n" \
  "ds_read_b128 v[72:75], %[xa0] offset:53248\n" \
  "s_add_u32 m0, %[xd], 2048\n" \
  "s_nop 0\n" \
  "global_load_lds_dwordx4 %[xs2], s[40:41]\n" \
  "v_mfma_f32_16x16x32_bf16 v[36:39], v[88:91], v[116:119], v[36:39]\n" \
  "ds_read_b64_tr_b16 v[108:109], %[wa3] offset:49152\n" \
  "v_mfma_f32_16x16x32_bf16 v[40:43], v[88:91], v[120:123], v[40:43]\n" \
  "ds_read_b64_tr_b16 v[110:111], %[wa3] offset:50176\n" \
  "s_add_u32 m0, %[xd], 3072\n" \
  "s_nop 0\n" \
  "global_load_lds_dwordx4 %[xs3], s[40:41]\n" \
  "v_mfma_f32_16x16x32_bf16 v[44:47], v[88:91], v[124:127], v[44:47]\n" \
  "ds_read_b128 v[76:79], %[xa0] offset:55296\n" \
  "v_mfma_f32_16x16x32_bf16 v[48:51], v[92:95], v[112:115], v[48:51]\n" \
  "s_add_u32 m0, %[wd], 0\n" \
  "s_nop 0\n" \
  "global_load_lds_dwordx4 %[ws0], s[42:43]\n" \
  "v_mfma_f32_16x16x32_bf16 v[52:55], v[92:95], v[116:119], v[52:55]\n" \
  "v_mfma_f32_16x16x32_bf16 v[56:59], v[92:95], v[120:123], v[56:59]\n" \
  "s_add_u32 m0, %[wd], 1024\n" \
  "s_nop 0\n" \
  "global_load_lds_dwordx4 %[ws1], s[42:43]\n" \
  "s_add_u32 s48, s48, 128\n" \
  "s_cmp_eq_u32 s48, 1280\n" \
  "s_cselect_b32 s48, 0, s48\n" \
  "s_add_u32 s40, s44, s48\n" \
  "s_addc_u32 s41, s45, 0\n" \
  "s_mul_i32 s49, s48, 1920\n" \
  "s_add_u32 s42, s46, s49\n" \
  "s_addc_u32 s43, s47, 0\n" \
  "v_mfma_f32_16x16x32_bf16 v[60:63], v[92:95], v[124:127], v[60:63]\n" \
  "s_waitcnt lgkmcnt(0)\n" \
  "v_mfma_f32_16x16x32_bf16 v[0:3], v[64:67], v[96:99], v[0:3]\n" \
  "ds_read_b64_tr_b16 v[112:113], %[wa0] offset:57344\n" \
  "v_mfma_f32_16x16x32_bf16 v[4:7], v[64:67], v[100:103], v[4:7]\n" \
  "ds_read_b64_tr_b16 v[114:115], %[wa0] offset:58368\n" \
  "v_mfma_f32_16x16x32_bf16 v[8:11], v[64:67], v[104:107], v[8:11]\n" \
  "ds_read_b128 v[80:83], %[xa1] offset:49152\n" \
  "v_mfma_f32_16x16x32_bf16 v[12:15], v[64:67], v[108:111], v[12:15]\n" \
  "ds_read_b64_tr_b16 v[116:117], %[wa1] offset:57344\n" \
  "v_mfma_f32_16x16x32_bf16 v[16:19], v[68:71], v[96:99], v[16:19]\n" \
  "ds_read_b64_tr_b16 v[118:119], %[wa1] offset:58368\n" \
  "v_mfma_f32_16x16x32_bf16 v[20:23], v[68:71], v[100:103], v[20:23]\n" \
  "ds_read_b128 v[84:87], %[xa1] offset:51200\n" \
  "v_mfma_f32_16x16x32_bf16 v[24:27], v[68:71], v[104:107], v[24:27]\n" \
  "ds_read_b64_tr_b16 v[120:121], %[wa2] offset:57344\n" \
  "v_mfma_f32_16x16x32_bf16 v[28:31], v[68:71], v[108:111], v[28:31]\n" \
  "ds_read_b64_tr_b16 v[122:123], %[wa2] offset:58368\n" \
  "v_mfma_f32_16x16x32_bf16 v[32:35], v[72:75], v[96:99], v[32:35]\n" \
  "ds_read_b128 v[88:91], %[xa1] offset:53248\n" \
  "v_mfma_f32_16x16x32_bf16 v[36:39], v[72:75], v[100:103], v[36:39]\n" \
  "ds_read_b64_tr_b16 v[124:125], %[wa3] offset:57344\n" \
  "v_mfma_f32_16x16x32_bf16 v[40:43], v[72:75], v[104:107], v[40:43]\n" \
  "ds_read_b64_tr_b16 v[126:127], %[wa3] offset:58368\n" \
  "v_mfma_f32_16x16x32_bf16 v[44:47], v[72:75], v[108:111], v[44:47]\n" \
  "ds_read_b128 v[92:95], %[xa1] offset:55296\n" \
  "v_mfma_f32_16x16x32_bf16 v[48:51], v[76:79], v[96:99], v[48:51]\n" \
  "v_mfma_f32_16x16x32_bf16 v[52:55], v[76:79], v[100:103], v[52:55]\n" \
  "v_mfma_f32_16x16x32_bf16 v[56:59], v[76:79], v[104:107], v[56:59]\n" \
  "v_mfma_f32_16x16x32_bf16 v[60:63], v[76:79], v[108:111], v[60:63]\n" \
  "s_waitcnt lgkmcnt(0)\n" \
  "s_waitcnt vmcnt(6)\n" \
  "s_barrier\n" \
  "v_mfma_f32_16x16x32_bf16 v[0:3], v[80:83], v[112:115], v[0:3]\n" \
  "ds_read_b64_tr_b16 v[96:97], v130 offset:0\n" \
  "v_mfma_f32_16x16x32_bf16 v[4:7], v[80:83], v[116:119], v[4:7]\n" \
  "ds_read_b64_tr_b16 v[98:99], v130 offset:1024\n" \
  "v_mfma_f32_16x16x32_bf16 v[8:11], v[80:83], v[120:123], v[8:11]\n" \
  "ds_read_b128 v[64:67], v128 offset:0\n" \
  "v_mfma_f32_16x16x32_bf16 v[12:15], v[80:83], v[124:127], v[12:15]\n" \
  "ds_read_b64_tr_b16 v[100:101], v131 offset:0\n" \
  "v_mfma_f32_16x16x32_bf16 v[16:19], v[84:87], v[112:115], v[16:19]\n" \
  "ds_read_b64_tr_b16 v[102:103], v131 offset:1024\n" \
  "s_add_u32 m0, %[xd], 49152\n" \
  "s_nop 0\n" \
  "global_load_lds_dwordx4 %[xs0], s[40:41]\n" \
  "v_mfma_f32_16x16x32_bf16 v[20:23], v[84:87], v[116:119], v[20:23]\n" \
  "ds_read_b128 v[68:71], v128 offset:2048\n" \
  "v_mfma_f32_16x16x32_bf16 v[24:27], v[84:87], v[120:123], v[24:27]\n" \
  "ds_read_b64_tr_b16 v[104:105], v132 offset:0\n" \
  "s_add_u32 m0, %[xd], 50176\n" \
  "s_nop 0\n" \
  "global_load_lds_dwordx4 %[xs1], s[40:41]\n" \
  "v_mfma_f32_16x16x32_bf16 v[28:31], v[84:87], v[124:127], v[28:31]\n" \
  "ds_read_b64_tr_b16 v[106:107], v132 offset:1024\n" \
  "v_mfma_f32_16x16x32_bf16 v[32:35], v[88:91], v[112:115], v[32:35]\n" \
  "ds_read_b128 v[72:75], v128 offset:4096\n" \
  "s_add_u32 m0, %[xd], 51200\n" \
  "s_nop 0\n" \
  "global_load_lds_dwordx4 %[xs2], s[40:41]\n" \
  "v_mfma_f32_16x16x32_bf16 v[36:39], v[88:91], v[116:119], v[36:39]\n" \
  "ds_read_b64_tr_b16 v[108:109], v133 offset:0\n" \
  "v_mfma_f32_16x16x32_bf16 v[40:43], v[88:91], v[120:123], v[40:43]\n" \
  "ds_read_b64_tr_b16 v[110:111], v133 offset:1024\n" \
  "s_add_u32 m0, %[xd], 52224\n" \
  "s_nop 0\n" \
  "global_load_lds_dwordx4 %[xs3], s[40:41]\n" \
  "v_mfma_f32_16x16x32_bf16 v[44:47], v[88:91], v[124:127], v[44:47]\n" \
  "ds_read_b128 v[76:79], v128 offset:6144\n" \
  "v_mfma_f32_16x16x32_bf16 v[48:51], v[92:95], v[112:115], v[48:51]\n" \
  "s_add_u32 m0, %[wd], 49152\n" \
  "s_nop 0\n" \
  "global_load_lds_dwordx4 %[ws0], s[42:43]\n" \
  "v_mfma_f32_16x16x32_bf16 v[52:55], v[92:95], v[116:119], v[52:55]\n" \
  "v_mfma_f32_16x16x32_bf16 v[56:59], v[92:95], v[120:123], v[56:59]\n" \
  "s_add_u32 m0, %[wd], 50176\n" \
  "s_nop 0\n" \
  "global_load_lds_dwordx4 %[ws1], s[42:43]\n" \
  "s_add_u32 s48, s48, 128\n" \
  "s_cmp_eq_u32 s48, 1280\n" \
  "s_cselect_b32 s48, 0, s48\n" \
  "s_add_u32 s40, s44, s48\n" \
  "s_addc_u32 s41, s45, 0\n" \
  "s_mul_i32 s49, s48, 1920\n" \
  "s_add_u32 s42, s46, s49\n" \
  "s_addc_u32 s43, s47, 0\n" \
  "v_mfma_f32_16x16x32_bf16 v[60:63], v[92:95], v[124:127], v[60:63]\n" \
  "s_waitcnt lgkmcnt(0)\n" \
  "v_mfma_f32_16x16x32_bf16 v[0:3], v[64:67], v[96:99], v[0:3]\n" \
  "ds_read_b64_tr_b16 v[112:113], v130 offset:8192\n" \
  "v_mfma_f32_16x16x32_bf16 v[4:7], v[64:67], v[100:103], v[4:7]\n" \
  "ds_read_b64_tr_b16 v[114:115], v130 offset:9216\n" \
  "v_mfma_f32_16x16x32_bf16 v[8:11], v[64:67], v[104:107], v[8:11]\n" \
  "ds_read_b128 v[80:83], v129 offset:0\n" \
  "v_mfma_f32_16x16x32_bf16 v[12:15], v[64:67], v[108:111], v[12:15]\n" \
  "ds_read_b64_tr_b16 v[116:117], v131 offset:8192\n" \
  "v_mfma_f32_16x16x32_bf16 v[16:19], v[68:71], v[96:99], v[16:19]\n" \
  "ds_read_b64_tr_b16 v[118:119], v131 offset:9216\n" \
  "v_mfma_f32_16x16x32_bf16 v[20:23], v[68:71], v[100:103], v[20:23]\n" \
  "ds_read_b128 v[84:87], v129 offset:2048\n" \
  "v_mfma_f32_16x16x32_bf16 v[24:27], v[68:71], v[104:107], v[24:27]\n" \
  "ds_read_b64_tr_b16 v[120:121], v132 offset:8192\n" \
  "v_mfma_f32_16x16x32_bf16 v[28:31], v[68:71], v[108:111], v[28:31]\n" \
  "ds_read_b64_tr_b16 v[122:123], v132 offset:9216\n" \
  "v_mfma_f32_16x16x32_bf16 v[32:35], v[72:75], v[96:99], v[32:35]\n" \
  "ds_read_b128 v[88:91], v129 offset:4096\n" \
  "v_mfma_f32_16x16x32_bf16 v[36:39], v[72:75], v[100:103], v[36:39]\n" \
  "ds_read_b64_tr_b16 v[124:125], v133 offset:8192\n" \
  "v_mfma_f32_16x16x32_bf16 v[40:43], v[72:75], v[104:107], v[40:43]\n" \
  "ds_read_b64_tr_b16 v[126:127], v133 offset:9216\n" \
  "v_mfma_f32_16x16x32_bf16 v[44:47], v[72:75], v[108:111], v[44:47]\n" \
  "ds_read_b128 v[92:95], v129 offset:6144\n" \
  "v_mfma_f32_16x16x32_bf16 v[48:51], v[76:79], v[96:99], v[48:51]\n" \
  "v_mfma_f32_16x16x32_bf16 v[52:55], v[76:79], v[100:103], v[52:55]\n" \
  "v_mfma_f32_16x16x32_bf16 v[56:59], v[76:79], v[104:107], v[56:59]\n" \
  "v_mfma_f32_16x16x32_bf16 v[60:63], v[76:79], v[108:111], v[60:63]\n" \
  "s_waitcnt lgkmcnt(0)\n" \
  "s_waitcnt vmcnt(6)\n" \
  "s_barrier\n" \
  "v_mfma_f32_16x16x32_bf16 v[0:3], v[80:83], v[112:115], v[0:3]\n" \
  "ds_read_b64_tr_b16 v[96:97], %[wa0] offset:0\n" \
  "v_mfma_f32_16x16x32_bf16 v[4:7], v[80:83], v[116:119], v[4:7]\n" \
  "ds_read_b64_tr_b16 v[98:99], %[wa0] offset:1024\n" \
  "v_mfma_f32_16x16x32_bf16 v[8:11], v[80:83], v[120:123], v[8:11]\n" \
  "ds_read_b128 v[64:67], %[xa0] offset:0\n" \
  "v_mfma_f32_16x16x32_bf16 v[12:15], v[80:83], v[124:127], v[12:15]\n" \
  "ds_read_b64_tr_b16 v[100:101], %[wa1] offset:0\n" \
  "v_mfma_f32_16x16x32_bf16 v[16:19], v[84:87], v[112:115], v[16:19]\n" \
  "ds_read_b64_tr_b16 v[102:103], %[wa1] offset:1024\n" \
  "s_add_u32 m0, %[xd], 98304\n" \
  "s_nop 0\n" \
  "global_load_lds_dwordx4 %[xs0], s[40:41]\n" \
  "v_mfma_f32_16x16x32_bf16 v[20:23], v[84:87], v[116:119], v[20:23]\n" \
  "ds_read_b128 v[68:71], %[xa0] offset:2048\n" \
  "v_mfma_f32_16x16x32_bf16 v[24:27], v[84:87], v[120:123], v[24:27]\n" \
  "ds_read_b64_tr_b16 v[104:105], %[wa2] offset:0\n" \
  "s_add_u32 m0, %[xd], 99328\n" \
  "s_nop 0\n" \
  "global_load_lds_dwordx4 %[xs1], s[40:41]\n" \
  "v_mfma_f32_16x16x32_bf16 v[28:31], v[84:87], v[124:127], v[28:31]\n" \
  "ds_read_b64_tr_b16 v[106:107], %[wa2] offset:1024\n" \
  "v_mfma_f32_16x16x32_bf16 v[32:35], v[88:91], v[112:115], v[32:35]\n" \
  "ds_read_b128 v[72:75], %[xa0] offset:4096\n" \
  "s_add_u32 m0, %[xd], 100352\n" \
  "s_nop 0\n" \
  "global_load_lds_dwordx4 %[xs2], s[40:41]\n" \
  "v_mfma_f32_16x16x32_bf16 v[36:39], v[88:91], v[116:119], v[36:39]\n" \
  "ds_read_b64_tr_b16 v[108:109], %[wa3] offset:0\n" \
  "v_mfma_f32_16x16x32_bf16 v[40:43], v[88:91], v[120:123], v[40:43]\n" \
  "ds_read_b64_tr_b16 v[110:111], %[wa3] offset:1024\n" \
  "s_add_u32 m0, %[xd], 101376\n" \
  "s_nop 0\n" \
  "global_load_lds_dwordx4 %[xs3], s[40:41]\n" \
  "v_mfma_f32_16x16x32_bf16 v[44:47], v[88:91], v[124:127], v[44:47]\n" \
  "ds_read_b128 v[76:79], %[xa0] offset:6144\n" \
  "v_mfma_f32_16x16x32_bf16 v[48:51], v[92:95], v[112:115], v[48:51]\n" \
  "s_add_u32 m0, %[wd], 98304\n" \
  "s_nop 0\n" \
  "global_load_lds_dwordx4 %[ws0], s[42:43]\n" \
  "v_mfma_f32_16x16x32_bf16 v[52:55], v[92:95], v[116:119], v[52:55]\n" \
  "v_mfma_f32_16x16x32_bf16 v[56:59], v[92:95], v[120:123], v[56:59]\n" \
  "s_add_u32 m0, %[wd], 99328\n" \
  "s_nop 0\n" \
  "global_load_lds_dwordx4 %[ws1], s[42:43]\n" \
  "s_add_u32 s48, s48, 128\n" \
  "s_cmp_eq_u32 s48, 1280\n" \
  "s_cselect_b32 s48, 0, s48\n" \
  "s_add_u32 s40, s44, s48\n" \
  "s_addc_u32 s41, s45, 0\n" \
  "s_mul_i32 s49, s48, 1920\n" \
  "s_add_u32 s42, s46, s49\n" \
  "s_addc_u32 s43, s47, 0\n" \
  "v_mfma_f32_16x16x32_bf16 v[60:63], v[92:95], v[124:127], v[60:63]\n" \
  "s_waitcnt lgkmcnt(0)\n" \
  "v_mfma_f32_16x16x32_bf16 v[0:3], v[64:67], v[96:99], v[0:3]\n" \
  "ds_read_b64_tr_b16 v[112:113], %[wa0] offset:8192\n" \
  "v_mfma_f32_16x16x32_bf16 v[4:7], v[64:67], v[100:103], v[4:7]\n" \
  "ds_read_b64_tr_b16 v[114:115], %[wa0] offset:9216\n" \
  "v_mfma_f32_16x16x32_bf16 v[8:11], v[64:67], v[104:107], v[8:11]\n" \
  "ds_read_b128 v[80:83], %[xa1] offset:0\n" \
  "v_mfma_f32_16x16x32_bf16 v[12:15], v[64:67], v[108:111], v[12:15]\n" \
  "ds_read_b64_tr_b16 v[116:117], %[wa1] offset:8192\n" \
  "v_mfma_f32_16x16x32_bf16 v[16:19], v[68:71], v[96:99], v[16:19]\n" \
  "ds_read_b64_tr_b16 v[118:119], %[wa1] offset:9216\n" \
  "v_mfma_f32_16x16x32_bf16 v[20:23], v[68:71], v[100:103], v[20:23]\n" \
  "ds_read_b128 v[84:87], %[xa1] offset:2048\n" \
  "v_mfma_f32_16x16x32_bf16 v[24:27], v[68:71], v[104:107], v[24:27]\n" \
  "ds_read_b64_tr_b16 v[120:121], %[wa2] offset:8192\n" \
  "v_mfma_f32_16x16x32_bf16 v[28:31], v[68:71], v[108:111], v[28:31]\n" \
  "ds_read_b64_tr_b16 v[122:123], %[wa2] offset:9216\n" \
  "v_mfma_f32_16x16x32_bf16 v[32:35], v[72:75], v[96:99], v[32:35]\n" \
  "ds_read_b128 v[88:91], %[xa1] offset:4096\n" \
  "v_mfma_f32_16x16x32_bf16 v[36:39], v[72:75], v[100:103], v[36:39]\n" \
  "ds_read_b64_tr_b16 v[124:125], %[wa3] offset:8192\n" \
  "v_mfma_f32_16x16x32_bf16 v[40:43], v[72:75], v[104:107], v[40:43]\n" \
  "ds_read_b64_tr_b16 v[126:127], %[wa3] offset:9216\n" \
  "v_mfma_f32_16x16x32_bf16 v[44:47], v[72:75], v[108:111], v[44:47]\n" \
  "ds_read_b128 v[92:95], %[xa1] offset:6144\n" \
  "v_mfma_f32_16x16x32_bf16 v[48:51], v[76:79], v[96:99], v[48:51]\n" \
  "v_mfma_f32_16x16x32_bf16 v[52:55], v[76:79], v[100:103], v[52:55]\n" \
  "v_mfma_f32_16x16x32_bf16 v[56:59], v[76:79], v[104:107], v[56:59]\n" \
  "v_mfma_f32_16x16x32_bf16 v[60:63], v[76:79], v[108:111], v[60:63]\n" \
  "s_waitcnt lgkmcnt(0)\n" \
  "s_waitcnt vmcnt(6)\n" \
  "s_barrier\n" \
  "v_mfma_f32_16x16x32_bf16 v[0:3], v[80:83], v[112:115], v[0:3]\n" \
  "ds_read_b64_tr_b16 v[96:97], %[wa0] offset:49152\n" \
  "v_mfma_f32_16x16x32_bf16 v[4:7], v[80:83], v[116:119], v[4:7]\n" \
  "ds_read_b64_tr_b16 v[98:99], %[wa0] offset:50176\n" \
  "v_mfma_f32_16x16x32_bf16 v[8:11], v[80:83], v[120:123], v[8:11]\n" \
  "ds_read_b128 v[64:67], %[xa0] offset:49152\n" \
  "v_mfma_f32_16x16x32_bf16 v[12:15], v[80:83], v[124:127], v[12:15]\n" \
  "ds_read_b64_tr_b16 v[100:101], %[wa1] offset:49152\n" \
  "v_mfma_f32_16x16x32_bf16 v[16:19], v[84:87], v[112:115], v[16:19]\n" \
  "ds_read_b64_tr_b16 v[102:103], %[wa1] offset:50176\n" \
  "s_add_u32 m0, %[xd], 0\n" \
  "s_nop 0\n" \
  "global_load_lds_dwordx4 %[xs0], s[40:41]\n" \
  "v_mfma_f32_16x16x32_bf16 v[20:23], v[84:87], v[116:119], v[20:23]\n" \
  "ds_read_b128 v[68:71], %[xa0] offset:51200\n" \
  "v_mfma_f32_16x16x32_bf16 v[24:27], v[84:87], v[120:123], v[24:27]\n" \
  "ds_read_b64_tr_b16 v[104:105], %[wa2] offset:49152\n" \
  "s_add_u32 m0, %[xd], 1024\n" \
  "s_nop 0\n" \
  "global_load_lds_dwordx4 %[xs1], s[40:41]\n" \
  "v_mfma_f32_16x16x32_bf16 v[28:31], v[84:87], v[124:127], v[28:31]\n" \
  "ds_read_b64_tr_b16 v[106:107], %[wa2] offset:50176\n" \
  "v_mfma_f32_16x16x32_bf16 v[32:35], v[88:91], v[112:115], v[32:35]\n" \
  "ds_read_b128 v[72:75], %[xa0] offset:53248\n" \
  "s_add_u32 m0, %[xd], 2048\n" \
  "s_nop 0\n" \
  "global_load_lds_dwordx4 %[xs2], s[40:41]\n" \
  "v_mfma_f32_16x16x32_bf16 v[36:39], v[88:91], v[116:119], v[36:39]\n" \
  "ds_read_b64_tr_b16 v[108:109], %[wa3] offset:49152\n" \
  "v_mfma_f32_16x16x32_bf16 v[40:43], v[88:91], v[120:123], v[40:43]\n" \
  "ds_read_b64_tr_b16 v[110:111], %[wa3] offset:50176\n" \
  "s_add_u32 m0, %[xd], 3072\n" \
  "s_nop 0\n" \
  "global_load_lds_dwordx4 %[xs3], s[40:41]\n" \
  "v_mfma_f32_16x16x32_bf16 v[44:47], v[88:91], v[124:127], v[44:47]\n" \
  "ds_read_b128 v[76:79], %[xa0] offset:55296\n" \
  "v_mfma_f32_16x16x32_bf16 v[48:51], v[92:95], v[112:115], v[48:51]\n" \
  "s_add_u32 m0, %[wd], 0\n" \
  "s_nop 0\n" \
  "global_load_lds_dwordx4 %[ws0], s[42:43]\n" \
  "v_mfma_f32_16x16x32_bf16 v[52:55], v[92:95], v[116:119], v[52:55]\n" \
  "v_mfma_f32_16x16x32_bf16 v[56:59], v[92:95], v[120:123], v[56:59]\n" \
  "s_add_u32 m0, %[wd], 1024\n" \
  "s_nop 0\n" \
  "global_load_lds_dwordx4 %[ws1], s[42:43]\n" \
  "s_add_u32 s48, s48, 128\n" \
  "s_cmp_eq_u32 s48, 1280\n" \
  "s_cselect_b32 s48, 0, s48\n" \
  "s_add_u32 s40, s44, s48\n" \
  "s_addc_u32 s41, s45, 0\n" \
  "s_mul_i32 s49, s48, 1920\n" \
  "s_add_u32 s42, s46, s49\n" \
  "s_addc_u32 s43, s47, 0\n" \
  "v_mfma_f32_16x16x32_bf16 v[60:63], v[92:95], v[124:127], v[60:63]\n" \
  "s_waitcnt lgkmcnt(0)\n" \
  "v_mfma_f32_16x16x32_bf16 v[0:3], v[64:67], v[96:99], v[0:3]\n" \
  "ds_read_b64_tr_b16 v[112:113], %[wa0] offset:57344\n" \
  "v_mfma_f32_16x16x32_bf16 v[4:7], v[64:67], v[100:103], v[4:7]\n" \
  "ds_read_b64_tr_b16 v[114:115], %[wa0] offset:58368\n" \
  "v_mfma_f32_16x16x32_bf16 v[8:11], v[64:67], v[104:107], v[8:11]\n" \
  "ds_read_b128 v[80:83], %[xa1] offset:49152\n" \
  "v_mfma_f32_16x16x32_bf16 v[12:15], v[64:67], v[108:111], v[12:15]\n" \
  "ds_read_b64_tr_b16 v[116:117], %[wa1] offset:57344\n" \
  "v_mfma_f32_16x16x32_bf16 v[16:19], v[68:71], v[96:99], v[16:19]\n" \
  "ds_read_b64_tr_b16 v[118:119], %[wa1] offset:58368\n" \
  "v_mfma_f32_16x16x32_bf16 v[20:23], v[68:71], v[100:103], v[20:23]\n" \
  "ds_read_b128 v[84:87], %[xa1] offset:51200\n" \
  "v_mfma_f32_16x16x32_bf16 v[24:27], v[68:71], v[104:107], v[24:27]\n" \
  "ds_read_b64_tr_b16 v[120:121], %[wa2] offset:57344\n" \
  "v_mfma_f32_16x16x32_bf16 v[28:31], v[68:71], v[108:111], v[28:31]\n" \
  "ds_read_b64_tr_b16 v[122:123], %[wa2] offset:58368\n" \
  "v_mfma_f32_16x16x32_bf16 v[32:35], v[72:75], v[96:99], v[32:35]\n" \
  "ds_read_b128 v[88:91], %[xa1] offset:53248\n" \
  "v_mfma_f32_16x16x32_bf16 v[36:39], v[72:75], v[100:103], v[36:39]\n" \
  "ds_read_b64_tr_b16 v[124:125], %[wa3] offset:57344\n" \
  "v_mfma_f32_16x16x32_bf16 v[40:43], v[72:75], v[104:107], v[40:43]\n" \
  "ds_read_b64_tr_b16 v[126:127], %[wa3] offset:58368\n" \
  "v_mfma_f32_16x16x32_bf16 v[44:47], v[72:75], v[108:111], v[44:47]\n" \
  "ds_read_b128 v[92:95], %[xa1] offset:55296\n" \
  "v_mfma_f32_16x16x32_bf16 v[48:51], v[76:79], v[96:99], v[48:51]\n" \
  "v_mfma_f32_16x16x32_bf16 v[52:55], v[76:79], v[100:103], v[52:55]\n" \
  "v_mfma_f32_16x16x32_bf16 v[56:59], v[76:79], v[104:107], v[56:59]\n" \
  "v_mfma_f32_16x16x32_bf16 v[60:63], v[76:79], v[108:111], v[60:63]\n" \
  "s_waitcnt lgkmcnt(0)\n" \
  "s_waitcnt vmcnt(6)\n" \
  "s_barrier\n" \
  "v_mfma_f32_16x16x32_bf16 v[0:3], v[80:83], v[112:115], v[0:3]\n" \
  "ds_read_b64_tr_b16 v[96:97], v130 offset:0\n" \
  "v_mfma_f32_16x16x32_bf16 v[4:7], v[80:83], v[116:119], v[4:7]\n" \
  "ds_read_b64_tr_b16 v[98:99], v130 offset:1024\n" \
  "v_mfma_f32_16x16x32_bf16 v[8:11], v[80:83], v[120:123], v[8:11]\n" \
  "ds_read_b128 v[64:67], v128 offset:0\n" \
  "v_mfma_f32_16x16x32_bf16 v[12:15], v[80:83], v[124:127], v[12:15]\n" \
  "ds_read_b64_tr_b16 v[100:101], v131 offset:0\n" \
  "v_mfma_f32_16x16x32_bf16 v[16:19], v[84:87], v[112:115], v[16:19]\n" \
  "ds_read_b64_tr_b16 v[102:103], v131 offset:1024\n" \
  "s_add_u32 m0, %[xd], 49152\n" \
  "s_nop 0\n" \
  "global_load_lds_dwordx4 %[xs0], s[40:41]\n" \
  "v_mfma_f32_16x16x32_bf16 v[20:23], v[84:87], v[116:119], v[20:23]\n" \
  "ds_read_b128 v[68:71], v128 offset:2048\n" \
  "v_mfma_f32_16x16x32_bf16 v[24:27], v[84:87], v[120:123], v[24:27]\n" \
  "ds_read_b64_tr_b16 v[104:105], v132 offset:0\n" \
  "s_add_u32 m0, %[xd], 50176\n" \
  "s_nop 0\n" \
  "global_load_lds_dwordx4 %[xs1], s[40:41]\n" \
  "v_mfma_f32_16x16x32_bf16 v[28:31], v[84:87], v[124:127], v[28:31]\n" \
  "ds_read_b64_tr_b16 v[106:107], v132 offset:1024\n" \
  "v_mfma_f32_16x16x32_bf16 v[32:35], v[88:91], v[112:115], v[32:35]\n" \
  "ds_read_b128 v[72:75], v128 offset:4096\n" \
  "s_add_u32 m0, %[xd], 51200\n" \
  "s_nop 0\n" \
  "global_load_lds_dwordx4 %[xs2], s[40:41]\n" \
  "v_mfma_f32_16x16x32_bf16 v[36:39], v[88:91], v[116:119], v[36:39]\n" \
  "ds_read_b64_tr_b16 v[108:109], v133 offset:0\n" \
  "v_mfma_f32_16x16x32_bf16 v[40:43], v[88:91], v[120:123], v[40:43]\n" \
  "ds_read_b64_tr_b16 v[110:111], v133 offset:1024\n" \
  "s_add_u32 m0, %[xd], 52224\n" \
  "s_nop 0\n" \
  "global_load_lds_dwordx4 %[xs3], s[40:41]\n" \
  "v_mfma_f32_16x16x32_bf16 v[44:47], v[88:91], v[124:127], v[44:47]\n" \
  "ds_read_b128 v[76:79], v128 offset:6144\n" \
  "v_mfma_f32_16x16x32_bf16 v[48:51], v[92:95], v[112:115], v[48:51]\n" \
  "s_add_u32 m0, %[wd], 49152\n" \
  "s_nop 0\n" \
  "global_load_lds_dwordx4 %[ws0], s[42:43]\n" \
  "v_mfma_f32_16x16x32_bf16 v[52:55], v[92:95], v[116:119], v[52:55]\n" \
  "v_mfma_f32_16x16x32_bf16 v[56:59], v[92:95], v[120:123], v[56:59]\n" \
  "s_add_u32 m0, %[wd], 50176\n" \
  "s_nop 0\n" \
  "global_load_lds_dwordx4 %[ws1], s[42:43]\n" \
  "s_add_u32 s48, s48, 128\n" \
  "s_cmp_eq_u32 s48, 1280\n" \
  "s_cselect_b32 s48, 0, s48\n" \
  "s_add_u32 s40, s44, s48\n" \
  "s_addc_u32 s41, s45, 0\n" \
  "s_mul_i32 s49, s48, 1920\n" \
  "s_add_u32 s42, s46, s49\n" \
  "s_addc_u32 s43, s47, 0\n" \
  "v_mfma_f32_16x16x32_bf16 v[60:63], v[92:95], v[124:127], v[60:63]\n" \
  "s_waitcnt lgkmcnt(0)\n" \
  "v_mfma_f32_16x16x32_bf16 v[0:3], v[64:67], v[96:99], v[0:3]\n" \
  "ds_read_b64_tr_b16 v[112:113], v130 offset:8192\n" \
  "v_mfma_f32_16x16x32_bf16 v[4:7], v[64:67], v[100:103], v[4:7]\n" \
  "ds_read_b64_tr_b16 v[114:115], v130 offset:9216\n" \
  "v_mfma_f32_16x16x32_bf16 v[8:11], v[64:67], v[104:107], v[8:11]\n" \
  "ds_read_b128 v[80:83], v129 offset:0\n" \
  "v_mfma_f32_16x16x32_bf16 v[12:15], v[64:67], v[108:111], v[12:15]\n" \
  "ds_read_b64_tr_b16 v[116:117], v131 offset:8192\n" \
  "v_mfma_f32_16x16x32_bf16 v[16:19], v[68:71], v[96:99], v[16:19]\n" \
  "ds_read_b64_tr_b16 v[118:119], v131 offset:9216\n" \
  "v_mfma_f32_16x16x32_bf16 v[20:23], v[68:71], v[100:103], v[20:23]\n" \
  "ds_read_b128 v[84:87], v129 offset:2048\n" \
  "v_mfma_f32_16x16x32_bf16 v[24:27], v[68:71], v[104:107], v[24:27]\n" \
  "ds_read_b64_tr_b16 v[120:121], v132 offset:8192\n" \
  "v_mfma_f32_16x16x32_bf16 v[28:31], v[68:71], v[108:111], v[28:31]\n" \
  "ds_read_b64_tr_b16 v[122:123], v132 offset:9216\n" \
  "v_mfma_f32_16x16x32_bf16 v[32:35], v[72:75], v[96:99], v[32:35]\n" \
  "ds_read_b128 v[88:91], v129 offset:4096\n" \
  "v_mfma_f32_16x16x32_bf16 v[36:39], v[72:75], v[100:103], v[36:39]\n" \
  "ds_read_b64_tr_b16 v[124:125], v133 offset:8192\n" \
  "v_mfma_f32_16x16x32_bf16 v[40:43], v[72:75], v[104:107], v[40:43]\n" \
  "ds_read_b64_tr_b16 v[126:127], v133 offset:9216\n" \
  "v_mfma_f32_16x16x32_bf16 v[44:47], v[72:75], v[108:111], v[44:47]\n" \
  "ds_read_b128 v[92:95], v129 offset:6144\n" \
  "v_mfma_f32_16x16x32_bf16 v[48:51], v[76:79], v[96:99], v[48:51]\n" \
  "v_mfma_f32_16x16x32_bf16 v[52:55], v[76:79], v[100:103], v[52:55]\n" \
  "v_mfma_f32_16x16x32_bf16 v[56:59], v[76:79], v[104:107], v[56:59]\n" \
  "v_mfma_f32_16x16x32_bf16 v[60:63], v[76:79], v[108:111], v[60:63]\n" \
  "s_waitcnt lgkmcnt(0)\n" \
  "s_waitcnt vmcnt(6)\n" \
  "s_barrier\n" \
  "v_mfma_f32_16x16x32_bf16 v[0:3], v[80:83], v[112:115], v[0:3]\n" \
  "ds_read_b64_tr_b16 v[96:97], %[wa0] offset:0\n" \
  "v_mfma_f32_16x16x32_bf16 v[4:7], v[80:83], v[116:119], v[4:7]\n" \
  "ds_read_b64_tr_b16 v[98:99], %[wa0] offset:1024\n" \
  "v_mfma_f32_16x16x32_bf16 v[8:11], v[80:83], v[120:123], v[8:11]\n" \
  "ds_read_b128 v[64:67], %[xa0] offset:0\n" \
  "v_mfma_f32_16x16x32_bf16 v[12:15], v[80:83], v[124:127], v[12:15]\n" \
  "ds_read_b64_tr_b16 v[100:101], %[wa1] offset:0\n" \
  "v_mfma_f32_16x16x32_bf16 v[16:19], v[84:87], v[112:115], v[16:19]\n" \
  "ds_read_b64_tr_b16 v[102:103], %[wa1] offset:1024\n" \
  "s_add_u32 m0, %[xd], 98304\n" \
  "s_nop 0\n" \
  "global_load_lds_dwordx4 %[xs0], s[40:41]\n" \
  "v_mfma_f32_16x16x32_bf16 v[20:23], v[84:87], v[116:119], v[20:23]\n" \
  "ds_read_b128 v[68:71], %[xa0] offset:2048\n" \
  "v_mfma_f32_16x16x32_bf16 v[24:27], v[84:87], v[120:123], v[24:27]\n" \
  "ds_read_b64_tr_b16 v[104:105], %[wa2] offset:0\n" \
  "s_add_u32 m0, %[xd], 99328\n" \
  "s_nop 0\n" \
  "global_load_lds_dwordx4 %[xs1], s[40:41]\n" \
  "v_mfma_f32_16x16x32_bf16 v[28:31], v[84:87], v[124:127], v[28:31]\n" \
  "ds_read_b64_tr_b16 v[106:107], %[wa2] offset:1024\n" \
  "v_mfma_f32_16x16x32_bf16 v[32:35], v[88:91], v[112:115], v[32:35]\n" \
  "ds_read_b128 v[72:75], %[xa0] offset:4096\n" \
  "s_add_u32 m0, %[xd], 100352\n" \
  "s_nop 0\n" \
  "global_load_lds_dwordx4 %[xs2], s[40:41]\n" \
  "v_mfma_f32_16x16x32_bf16 v[36:39], v[88:91], v[116:119], v[36:39]\n" \
  "ds_read_b64_tr_b16 v[108:109], %[wa3] offset:0\n" \
  "v_mfma_f32_16x16x32_bf16 v[40:43], v[88:91], v[120:123], v[40:43]\n" \
  "ds_read_b64_tr_b16 v[110:111], %[wa3] offset:1024\n" \
  "s_add_u32 m0, %[xd], 101376\n" \
  "s_nop 0\n" \
  "global_load_lds_dwordx4 %[xs3], s[40:41]\n" \
  "v_mfma_f32_16x16x32_bf16 v[44:47], v[88:91], v[124:127], v[44:47]\n" \
  "ds_read_b128 v[76:79], %[xa0] offset:6144\n" \
  "v_mfma_f32_16x16x32_bf16 v[48:51], v[92:95], v[112:115], v[48:51]\n" \
  "s_add_u32 m0, %[wd], 98304\n" \
  "s_nop 0\n" \
  "global_load_lds_dwordx4 %[ws0], s[42:43]\n" \
  "v_mfma_f32_16x16x32_bf16 v[52:55], v[92:95], v[116:119], v[52:55]\n" \
  "v_mfma_f32_16x16x32_bf16 v[56:59], v[92:95], v[120:123], v[56:59]\n" \
  "s_add_u32 m0, %[wd], 99328\n" \
  "s_nop 0\n" \
  "global_load_lds_dwordx4 %[ws1], s[42:43]\n" \
  "s_add_u32 s48, s48, 128\n" \
  "s_cmp_eq_u32 s48, 1280\n" \
  "s_cselect_b32 s48, 0, s48\n" \
  "s_add_u32 s40, s44, s48\n" \
  "s_addc_u32 s41, s45, 0\n" \
  "s_mul_i32 s49, s48, 1920\n" \
  "s_add_u32 s42, s46, s49\n" \
  "s_addc_u32 s43, s47, 0\n" \
  "v_mfma_f32_16x16x32_bf16 v[60:63], v[92:95], v[124:127], v[60:63]\n" \
  "s_waitcnt lgkmcnt(0)\n" \
  "v_mfma_f32_16x16x32_bf16 v[0:3], v[64:67], v[96:99], v[0:3]\n" \
  "ds_read_b64_tr_b16 v[112:113], %[wa0] offset:8192\n" \
  "v_mfma_f32_16x16x32_bf16 v[4:7], v[64:67], v[100:103], v[4:7]\n" \
  "ds_read_b64_tr_b16 v[114:115], %[wa0] offset:9216\n" \
  "v_mfma_f32_16x16x32_bf16 v[8:11], v[64:67], v[104:107], v[8:11]\n" \
  "ds_read_b128 v[80:83], %[xa1] offset:0\n" \
  "v_mfma_f32_16x16x32_bf16 v[12:15], v[64:67], v[108:111], v[12:15]\n" \
  "ds_read_b64_tr_b16 v[116:117], %[wa1] offset:8192\n" \
  "v_mfma_f32_16x16x32_bf16 v[16:19], v[68:71], v[96:99], v[16:19]\n" \
  "ds_read_b64_tr_b16 v[118:119], %[wa1] offset:9216\n" \
  "v_mfma_f32_16x16x32_bf16 v[20:23], v[68:71], v[100:103], v[20:23]\n" \
  "ds_read_b128 v[84:87], %[xa1] offset:2048\n" \
  "v_mfma_f32_16x16x32_bf16 v[24:27], v[68:71], v[104:107], v[24:27]\n" \
  "ds_read_b64_tr_b16 v[120:121], %[wa2] offset:8192\n" \
  "v_mfma_f32_16x16x32_bf16 v[28:31], v[68:71], v[108:111], v[28:31]\n" \
  "ds_read_b64_tr_b16 v[122:123], %[wa2] offset:9216\n" \
  "v_mfma_f32_16x16x32_bf16 v[32:35], v[72:75], v[96:99], v[32:35]\n" \
  "ds_read_b128 v[88:91], %[xa1] offset:4096\n" \
  "v_mfma_f32_16x16x32_bf16 v[36:39], v[72:75], v[100:103], v[36:39]\n" \
  "ds_read_b64_tr_b16 v[124:125], %[wa3] offset:8192\n" \
  "v_mfma_f32_16x16x32_bf16 v[40:43], v[72:75], v[104:107], v[40:43]\n" \
  "ds_read_b64_tr_b16 v[126:127], %[wa3] offset:9216\n" \
  "v_mfma_f32_16x16x32_bf16 v[44:47], v[72:75], v[108:111], v[44:47]\n" \
  "ds_read_b128 v[92:95], %[xa1] offset:6144\n" \
  "v_mfma_f32_16x16x32_bf16 v[48:51], v[76:79], v[96:99], v[48:51]\n" \
  "v_mfma_f32_16x16x32_bf16 v[52:55], v[76:79], v[100:103], v[52:55]\n" \
  "v_mfma_f32_16x16x32_bf16 v[56:59], v[76:79], v[104:107], v[56:59]\n" \
  "v_mfma_f32_16x16x32_bf16 v[60:63], v[76:79], v[108:111], v[60:63]\n" \
  "s_waitcnt lgkmcnt(0)\n" \
  "s_waitcnt vmcnt(6)\n" \
  "s_barrier\n" \
  "v_mfma_f32_16x16x32_bf16 v[0:3], v[80:83], v[112:115], v[0:3]\n" \
  "ds_read_b64_tr_b16 v[96:97], %[wa0] offset:49152\n" \
  "v_mfma_f32_16x16x32_bf16 v[4:7], v[80:83], v[116:119], v[4:7]\n" \
  "ds_read_b64_tr_b16 v[98:99], %[wa0] offset:50176\n" \
  "v_mfma_f32_16x16x32_bf16 v[8:11], v[80:83], v[120:123], v[8:11]\n" \
  "ds_read_b128 v[64:67], %[xa0] offset:49152\n" \
  "v_mfma_f32_16x16x32_bf16 v[12:15], v[80:83], v[124:127], v[12:15]\n" \
  "ds_read_b64_tr_b16 v[100:101], %[wa1] offset:49152\n" \
  "v_mfma_f32_16x16x32_bf16 v[16:19], v[84:87], v[112:115], v[16:19]\n" \
  "ds_read_b64_tr_b16 v[102:103], %[wa1] offset:50176\n" \
  "s_add_u32 m0, %[xd], 0\n" \
  "s_nop 0\n" \
  "global_load_lds_dwordx4 %[xs0], s[40:41]\n" \
  "v_mfma_f32_16x16x32_bf16 v[20:23], v[84:87], v[116:119], v[20:23]\n" \
  "ds_read_b128 v[68:71], %[xa0] offset:51200\n" \
  "v_mfma_f32_16x16x32_bf16 v[24:27], v[84:87], v[120:123], v[24:27]\n" \
  "ds_read_b64_tr_b16 v[104:105], %[wa2] offset:49152\n" \
  "s_add_u32 m0, %[xd], 1024\n" \
  "s_nop 0\n" \
  "global_load_lds_dwordx4 %[xs1], s[40:41]\n" \
  "v_mfma_f32_16x16x32_bf16 v[28:31], v[84:87], v[124:127], v[28:31]\n" \
  "ds_read_b64_tr_b16 v[106:107], %[wa2] offset:50176\n" \
  "v_mfma_f32_16x16x32_bf16 v[32:35], v[88:91], v[112:115], v[32:35]\n" \
  "ds_read_b128 v[72:75], %[xa0] offset:53248\n" \
  "s_add_u32 m0, %[xd], 2048\n" \
  "s_nop 0\n" \
  "global_load_lds_dwordx4 %[xs2], s[40:41]\n" \
  "v_mfma_f32_16x16x32_bf16 v[36:39], v[88:91], v[116:119], v[36:39]\n" \
  "ds_read_b64_tr_b16 v[108:109], %[wa3] offset:49152\n" \
  "v_mfma_f32_16x16x32_bf16 v[40:43], v[88:91], v[120:123], v[40:43]\n" \
  "ds_read_b64_tr_b16 v[110:111], %[wa3] offset:50176\n" \
  "s_add_u32 m0, %[xd], 3072\n" \
  "s_nop 0\n" \
  "global_load_lds_dwordx4 %[xs3], s[40:41]\n" \
  "v_mfma_f32_16x16x32_bf16 v[44:47], v[88:91], v[124:127], v[44:47]\n" \
  "ds_read_b128 v[76:79], %[xa0] offset:55296\n" \
  "v_mfma_f32_16x16x32_bf16 v[48:51], v[92:95], v[112:115], v[48:51]\n" \
  "s_add_u32 m0, %[wd], 0\n" \
  "s_nop 0\n" \
  "global_load_lds_dwordx4 %[ws0], s[42:43]\n" \
  "v_mfma_f32_16x16x32_bf16 v[52:55], v[92:95], v[116:119], v[52:55]\n" \
  "v_mfma_f32_16x16x32_bf16 v[56:59], v[92:95], v[120:123], v[56:59]\n" \
  "s_add_u32 m0, %[wd], 1024\n" \
  "s_nop 0\n" \
  "global_load_lds_dwordx4 %[ws1], s[42:43]\n" \
  "s_add_u32 s48, s48, 128\n" \
  "s_cmp_eq_u32 s48, 1280\n" \
  "s_cselect_b32 s48, 0, s48\n" \
  "s_add_u32 s40, s44, s48\n" \
  "s_addc_u32 s41, s45, 0\n" \
  "s_mul_i32 s49, s48, 1920\n" \
  "s_add_u32 s42, s46, s49\n" \
  "s_addc_u32 s43, s47, 0\n" \
  "v_mfma_f32_16x16x32_bf16 v[60:63], v[92:95], v[124:127], v[60:63]\n" \
  "s_waitcnt lgkmcnt(0)\n" \
  "global_load_dword %[rv], %[ro], %[wsb] sc1\n" \
  "v_mfma_f32_16x16x32_bf16 v[0:3], v[64:67], v[96:99], v[0:3]\n" \
  "ds_read_b64_tr_b16 v[112:113], %[wa0] offset:57344\n" \
  "v_mfma_f32_16x16x32_bf16 v[4:7], v[64:67], v[100:103], v[4:7]\n" \
  "ds_read_b64_tr_b16 v[114:115], %[wa0] offset:58368\n" \
  "v_mfma_f32_16x16x32_bf16 v[8:11], v[64:67], v[104:107], v[8:11]\n" \
  "ds_read_b128 v[80:83], %[xa1] offset:49152\n" \
  "v_mfma_f32_16x16x32_bf16 v[12:15], v[64:67], v[108:111], v[12:15]\n" \
  "ds_read_b64_tr_b16 v[116:117], %[wa1] offset:57344\n" \
  "v_mfma_f32_16x16x32_bf16 v[16:19], v[68:71], v[96:99], v[16:19]\n" \
  "ds_read_b64_tr_b16 v[118:119], %[wa1] offset:58368\n" \
  "v_mfma_f32_16x16x32_bf16 v[20:23], v[68:71], v[100:103], v[20:23]\n" \
  "ds_read_b128 v[84:87], %[xa1] offset:51200\n" \
  "v_mfma_f32_16x16x32_bf16 v[24:27], v[68:71], v[104:107], v[24:27]\n" \
  "ds_read_b64_tr_b16 v[120:121], %[wa2] offset:57344\n" \
  "v_mfma_f32_16x16x32_bf16 v[28:31], v[68:71], v[108:111], v[28:31]\n" \
  "ds_read_b64_tr_b16 v[122:123], %[wa2] offset:58368\n" \
  "v_mfma_f32_16x16x32_bf16 v[32:35], v[72:75], v[96:99], v[32:35]\n" \
  "ds_read_b128 v[88:91], %[xa1] offset:53248\n" \
  "v_mfma_f32_16x16x32_bf16 v[36:39], v[72:75], v[100:103], v[36:39]\n" \
  "ds_read_b64_tr_b16 v[124:125], %[wa3] offset:57344\n" \
  "v_mfma_f32_16x16x32_bf16 v[40:43], v[72:75], v[104:107], v[40:43]\n" \
  "ds_read_b64_tr_b16 v[126:127], %[wa3] offset:58368\n" \
  "v_mfma_f32_16x16x32_bf16 v[44:47], v[72:75], v[108:111], v[44:47]\n" \
  "ds_read_b128 v[92:95], %[xa1] offset:55296\n" \
  "v_mfma_f32_16x16x32_bf16 v[48:51], v[76:79], v[96:99], v[48:51]\n" \
  "v_mfma_f32_16x16x32_bf16 v[52:55], v[76:79], v[100:103], v[52:55]\n" \
  "v_mfma_f32_16x16x32_bf16 v[56:59], v[76:79], v[104:107], v[56:59]\n" \
  "v_mfma_f32_16x16x32_bf16 v[60:63], v[76:79], v[108:111], v[60:63]\n" \
  "s_waitcnt lgkmcnt(0)\n" \
  "s_waitcnt vmcnt(7)\n" \
  "s_barrier\n" \
  "v_mfma_f32_16x16x32_bf16 v[0:3], v[80:83], v[112:115], v[0:3]\n" \
  "ds_read_b64_tr_b16 v[96:97], v130 offset:0\n" \
  "v_mfma_f32_16x16x32_bf16 v[4:7], v[80:83], v[116:119], v[4:7]\n" \
  "ds_read_b64_tr_b16 v[98:99], v130 offset:1024\n" \
  "v_mfma_f32_16x16x32_bf16 v[8:11], v[80:83], v[120:123], v[8:11]\n" \
  "ds_read_b128 v[64:67], v128 offset:0\n" \
  "v_mfma_f32_16x16x32_bf16 v[12:15], v[80:83], v[124:127], v[12:15]\n" \
  "ds_read_b64_tr_b16 v[100:101], v131 offset:0\n" \
  "v_mfma_f32_16x16x32_bf16 v[16:19], v[84:87], v[112:115], v[16:19]\n" \
  "ds_read_b64_tr_b16 v[102:103], v131 offset:1024\n" \
  "v_mfma_f32_16x16x32_bf16 v[20:23], v[84:87], v[116:119], v[20:23]\n" \
  "ds_read_b128 v[68:71], v128 offset:2048\n" \
  "v_mfma_f32_16x16x32_bf16 v[24:27], v[84:87], v[120:123], v[24:27]\n" \
  "ds_read_b64_tr_b16 v[104:105], v132 offset:0\n" \
  "v_mfma_f32_16x16x32_bf16 v[28:31], v[84:87], v[124:127], v[28:31]\n" \
  "ds_read_b64_tr_b16 v[106:107], v132 offset:1024\n" \
  "v_mfma_f32_16x16x32_bf16 v[32:35], v[88:91], v[112:115], v[32:35]\n" \
  "ds_read_b128 v[72:75], v128 offset:4096\n" \
  "v_mfma_f32_16x16x32_bf16 v[36:39], v[88:91], v[116:119], v[36:39]\n" \
  "ds_read_b64_tr_b16 v[108:109], v133 offset:0\n" \
  "v_mfma_f32_16x16x32_bf16 v[40:43], v[88:91], v[120:123], v[40:43]\n" \
  "ds_read_b64_tr_b16 v[110:111], v133 offset:1024\n" \
  "v_mfma_f32_16x16x32_bf16 v[44:47], v[88:91], v[124:127], v[44:47]\n" \
  "ds_read_b128 v[76:79], v128 offset:6144\n" \
  "v_mfma_f32_16x16x32_bf16 v[48:51], v[92:95], v[112:115], v[48:51]\n" \
  "v_mfma_f32_16x16x32_bf16 v[52:55], v[92:95], v[116:119], v[52:55]\n" \
  "v_mfma_f32_16x16x32_bf16 v[56:59], v[92:95], v[120:123], v[56:59]\n" \
  "v_mfma_f32_16x16x32_bf16 v[60:63], v[92:95], v[124:127], v[60:63]\n" \
  "s_waitcnt lgkmcnt(0)\n" \
  "v_mfma_f32_16x16x32_bf16 v[0:3], v[64:67], v[96:99], v[0:3]\n" \
  "ds_read_b64_tr_b16 v[112:113], v130 offset:8192\n" \
  "v_mfma_f32_16x16x32_bf16 v[4:7], v[64:67], v[100:103], v[4:7]\n" \
  "ds_read_b64_tr_b16 v[114:115], v130 offset:9216\n" \
  "v_mfma_f32_16x16x32_bf16 v[8:11], v[64:67], v[104:107], v[8:11]\n" \
  "ds_read_b128 v[80:83], v129 offset:0\n" \
  "v_mfma_f32_16x16x32_bf16 v[12:15], v[64:67], v[108:111], v[12:15]\n" \
  "ds_read_b64_tr_b16 v[116:117], v131 offset:8192\n" \
  "v_mfma_f32_16x16x32_bf16 v[16:19], v[68:71], v[96:99], v[16:19]\n" \
  "ds_read_b64_tr_b16 v[118:119], v131 offset:9216\n" \
  "v_mfma_f32_16x16x32_bf16 v[20:23], v[68:71], v[100:103], v[20:23]\n" \
  "ds_read_b128 v[84:87], v129 offset:2048\n" \
  "v_mfma_f32_16x16x32_bf16 v[24:27], v[68:71], v[104:107], v[24:27]\n" \
  "ds_read_b64_tr_b16 v[120:121], v132 offset:8192\n" \
  "v_mfma_f32_16x16x32_bf16 v[28:31], v[68:71], v[108:111], v[28:31]\n" \
  "ds_read_b64_tr_b16 v[122:123], v132 offset:9216\n" \
  "v_mfma_f32_16x16x32_bf16 v[32:35], v[72:75], v[96:99], v[32:35]\n" \
  "ds_read_b128 v[88:91], v129 offset:4096\n" \
  "v_mfma_f32_16x16x32_bf16 v[36:39], v[72:75], v[100:103], v[36:39]\n" \
  "ds_read_b64_tr_b16 v[124:125], v133 offset:8192\n" \
  "v_mfma_f32_16x16x32_bf16 v[40:43], v[72:75], v[104:107], v[40:43]\n" \
  "ds_read_b64_tr_b16 v[126:127], v133 offset:9216\n" \
  "v_mfma_f32_16x16x32_bf16 v[44:47], v[72:75], v[108:111], v[44:47]\n" \
  "ds_read_b128 v[92:95], v129 offset:6144\n" \
  "v_mfma_f32_16x16x32_bf16 v[48:51], v[76:79], v[96:99], v[48:51]\n" \
  "v_mfma_f32_16x16x32_bf16 v[52:55], v[76:79], v[100:103], v[52:55]\n" \
  "v_mfma_f32_16x16x32_bf16 v[56:59], v[76:79], v[104:107], v[56:59]\n" \
  "v_mfma_f32_16x16x32_bf16 v[60:63], v[76:79], v[108:111], v[60:63]\n" \
  "s_waitcnt lgkmcnt(0)\n" \
  "s_waitcnt vmcnt(0)\n" \
  "s_barrier\n" \
  "s_cmp_lg_u32 %[w], 7\n" \
  "s_cbranch_scc1 L_noack_%=\n" \
  "v_readfirstlane_b32 s49, %[rv]\n" \
  "s_cmp_lg_u32 s49, 1\n" \
  "s_cbranch_scc1 L_noack_%=\n" \
  "s_mov_b64 exec, 1\n" \
  "global_store_dword %[ro], %[rv], %[wsb] offset:4 sc1\n" \
  "s_mov_b64 exec, -1\n" \
  "L_noack_%=:\n" \
  "v_mfma_f32_16x16x32_bf16 v[0:3], v[80:83], v[112:115], v[0:3]\n" \
  "ds_read_b64_tr_b16 v[96:97], %[wa0] offset:0\n" \
  "v_mfma_f32_16x16x32_bf16 v[4:7], v[80:83], v[116:119], v[4:7]\n" \
  "ds_read_b64_tr_b16 v[98:99], %[wa0] offset:1024\n" \
  "v_mfma_f32_16x16x32_bf16 v[8:11], v[80:83], v[120:123], v[8:11]\n" \
  "ds_read_b128 v[64:67], %[xa0] offset:0\n" \
  "v_mfma_f32_16x16x32_bf16 v[12:15], v[80:83], v[124:127], v[12:15]\n" \
  "ds_read_b64_tr_b16 v[100:101], %[wa1] offset:0\n" \
  "v_mfma_f32_16x16x32_bf16 v[16:19], v[84:87], v[112:115], v[16:19]\n" \
  "ds_read_b64_tr_b16 v[102:103], %[wa1] offset:1024\n" \
  "v_mfma_f32_16x16x32_bf16 v[20:23], v[84:87], v[116:119], v[20:23]\n" \
  "ds_read_b128 v[68:71], %[xa0] offset:2048\n" \
  "v_mfma_f32_16x16x32_bf16 v[24:27], v[84:87], v[120:123], v[24:27]\n" \
  "ds_read_b64_tr_b16 v[104:105], %[wa2] offset:0\n" \
  "v_mfma_f32_16x16x32_bf16 v[28:31], v[84:87], v[124:127], v[28:31]\n" \
  "ds_read_b64_tr_b16 v[106:107], %[wa2] offset:1024\n" \
  "v_mfma_f32_16x16x32_bf16 v[32:35], v[88:91], v[112:115], v[32:35]\n" \
  "ds_read_b128 v[72:75], %[xa0] offset:4096\n" \
  "v_mfma_f32_16x16x32_bf16 v[36:39], v[88:91], v[116:119], v[36:39]\n" \
  "ds_read_b64_tr_b16 v[108:109], %[wa3] offset:0\n" \
  "v_mfma_f32_16x16x32_bf16 v[40:43], v[88:91], v[120:123], v[40:43]\n" \
  "ds_read_b64_tr_b16 v[110:111], %[wa3] offset:1024\n" \
  "v_mfma_f32_16x16x32_bf16 v[44:47], v[88:91], v[124:127], v[44:47]\n" \
  "ds_read_b128 v[76:79], %[xa0] offset:6144\n" \
  "v_mfma_f32_16x16x32_bf16 v[48:51], v[92:95], v[112:115], v[48:51]\n" \
  "v_mfma_f32_16x16x32_bf16 v[52:55], v[92:95], v[116:119], v[52:55]\n" \
  "v_mfma_f32_16x16x32_bf16 v[56:59], v[92:95], v[120:123], v[56:59]\n" \
  "v_mfma_f32_16x16x32_bf16 v[60:63], v[92:95], v[124:127], v[60:63]\n" \
  "s_waitcnt lgkmcnt(0)\n" \
  "v_mfma_f32_16x16x32_bf16 v[0:3], v[64:67], v[96:99], v[0:3]\n" \
  "ds_read_b64_tr_b16 v[112:113], %[wa0] offset:8192\n" \
  "ds_read_b64_tr_b16 v[114:115], %[wa0] offset:9216\n" \
  "v_mfma_f32_16x16x32_bf16 v[16:19], v[68:71], v[96:99], v[16:19]\n" \
  "ds_read_b128 v[80:83], %[xa1] offset:0\n" \
  "ds_read_b64_tr_b16 v[116:117], %[wa1] offset:8192\n" \
  "v_mfma_f32_16x16x32_bf16 v[32:35], v[72:75], v[96:99], v[32:35]\n" \
  "ds_read_b64_tr_b16 v[118:119], %[wa1] offset:9216\n" \
  "ds_read_b128 v[84:87], %[xa1] offset:2048\n" \
  "v_mfma_f32_16x16x32_bf16 v[48:51], v[76:79], v[96:99], v[48:51]\n" \
  "ds_read_b64_tr_b16 v[120:121], %[wa2] offset:8192\n" \
  "ds_read_b64_tr_b16 v[122:123], %[wa2] offset:9216\n" \
  "v_mfma_f32_16x16x32_bf16 v[4:7], v[64:67], v[100:103], v[4:7]\n" \
  "ds_read_b128 v[88:91], %[xa1] offset:4096\n" \
  "ds_read_b64_tr_b16 v[124:125], %[wa3] offset:8192\n" \
  "v_mfma_f32_16x16x32_bf16 v[20:23], v[68:71], v[100:103], v[20:23]\n" \
  "ds_read_b64_tr_b16 v[126:127], %[wa3] offset:9216\n" \
  "ds_read_b128 v[92:95], %[xa1] offset:6144\n" \
  "v_mfma_f32_16x16x32_bf16 v[36:39], v[72:75], v[100:103], v[36:39]\n" \
  "v_mfma_f32_16x16x32_bf16 v[52:55], v[76:79], v[100:103], v[52:55]\n" \
  "s_waitcnt lgkmcnt(0)\n" \
  "v_mfma_f32_16x16x32_bf16 v[0:3], v[80:83], v[112:115], v[0:3]\n" \
  "v_mfma_f32_16x16x32_bf16 v[16:19], v[84:87], v[112:115], v[16:19]\n" \
  "v_mfma_f32_16x16x32_bf16 v[32:35], v[88:91], v[112:115], v[32:35]\n" \
  "v_mfma_f32_16x16x32_bf16 v[48:51], v[92:95], v[112:115], v[48:51]\n" \
  "v_mfma_f32_16x16x32_bf16 v[4:7], v[80:83], v[116:119], v[4:7]\n" \
  "v_mfma_f32_16x16x32_bf16 v[20:23], v[84:87], v[116:119], v[20:23]\n" \
  "s_nop 5\n" \
  "v_cvt_pk_bf16_f32 v0, v0, v1\n" \
  "v_cvt_pk_bf16_f32 v1, v2, v3\n" \
  "v_cvt_pk_bf16_f32 v2, v16, v17\n" \
  "v_cvt_pk_bf16_f32 v3, v18, v19\n" \
  "global_store_dwordx4 %[ea0], v[0:3], %[wsb] offset:0 sc0 sc1\n" \
  "v_cvt_pk_bf16_f32 v32, v32, v33\n" \
  "v_mfma_f32_16x16x32_bf16 v[36:39], v[88:91], v[116:119], v[36:39]\n" \
  "v_cvt_pk_bf16_f32 v33, v34, v35\n" \
  "v_cvt_pk_bf16_f32 v34, v48, v49\n" \
  "v_cvt_pk_bf16_f32 v35, v50, v51\n" \
  "global_store_dwordx4 %[ea0], v[32:35], %[wsb] offset:1280 sc0 sc1\n" \
  "v_mfma_f32_16x16x32_bf16 v[52:55], v[92:95], v[116:119], v[52:55]\n" \
  "v_mfma_f32_16x16x32_bf16 v[8:11], v[64:67], v[104:107], v[8:11]\n" \
  "v_mfma_f32_16x16x32_bf16 v[24:27], v[68:71], v[104:107], v[24:27]\n" \
  "s_nop 5\n" \
  "v_cvt_pk_bf16_f32 v4, v4, v5\n" \
  "v_cvt_pk_bf16_f32 v5, v6, v7\n" \
  "v_cvt_pk_bf16_f32 v6, v20, v21\n" \
  "v_cvt_pk_bf16_f32 v7, v22, v23\n" \
  "global_store_dwordx4 %[ea1], v[4:7], %[wsb] offset:0 sc0 sc1\n" \
  "v_cvt_pk_bf16_f32 v36, v36, v37\n" \
  "v_mfma_f32_16x16x32_bf16 v[40:43], v[72:75], v[104:107], v[40:43]\n" \
  "v_cvt_pk_bf16_f32 v37, v38, v39\n" \
  "v_cvt_pk_bf16_f32 v38, v52, v53\n" \
  "v_cvt_pk_bf16_f32 v39, v54, v55\n" \
  "global_store_dwordx4 %[ea1], v[36:39], %[wsb] offset:1280 sc0 sc1\n" \
  "v_mfma_f32_16x16x32_bf16 v[56:59], v[76:79], v[104:107], v[56:59]\n" \
  "v_mfma_f32_16x16x32_bf16 v[8:11], v[80:83], v[120:123], v[8:11]\n" \
  "v_mfma_f32_16x16x32_bf16 v[24:27], v[84:87], v[120:123], v[24:27]\n" \
  "v_mfma_f32_16x16x32_bf16 v[40:43], v[88:91], v[120:123], v[40:43]\n" \
  "v_mfma_f32_16x16x32_bf16 v[56:59], v[92:95], v[120:123], v[56:59]\n" \
  "v_mfma_f32_16x16x32_bf16 v[12:15], v[64:67], v[108:111], v[12:15]\n" \
  "v_mfma_f32_16x16x32_bf16 v[28:31], v[68:71], v[108:111], v[28:31]\n" \
  "s_nop 5\n" \
  "v_cvt_pk_bf16_f32 v8, v8, v9\n" \
  "v_cvt_pk_bf16_f32 v9, v10, v11\n" \
  "v_cvt_pk_bf16_f32 v10, v24, v25\n" \
  "v_cvt_pk_bf16_f32 v11, v26, v27\n" \
  "global_store_dwordx4 %[ea2], v[8:11], %[wsb] offset:0 sc0 sc1\n" \
  "v_cvt_pk_bf16_f32 v40, v40, v41\n" \
  "v_mfma_f32_16x16x32_bf16 v[44:47], v[72:75], v[108:111], v[44:47]\n" \
  "v_cvt_pk_bf16_f32 v41, v42, v43\n" \
  "v_cvt_pk_bf16_f32 v42, v56, v57\n" \
  "v_cvt_pk_bf16_f32 v43, v58, v59\n" \
  "global_store_dwordx4 %[ea2], v[40:43], %[wsb] offset:1280 sc0 sc1\n" \
  "v_mfma_f32_16x16x32_bf16 v[60:63], v[76:79], v[108:111], v[60:63]\n" \
  "v_mfma_f32_16x16x32_bf16 v[12:15], v[80:83], v[124:127], v[12:15]\n" \
  "v_mfma_f32_16x16x32_bf16 v[28:31], v[84:87], v[124:127], v[28:31]\n" \
  "v_mfma_f32_16x16x32_bf16 v[44:47], v[88:91], v[124:127], v[44:47]\n" \
  "v_mfma_f32_16x16x32_bf16 v[60:63], v[92:95], v[124:127], v[60:63]\n" \
  "s_nop 7\n" \
  "v_cvt_pk_bf16_f32 v12, v12, v13\n" \
  "v_cvt_pk_bf16_f32 v13, v14, v15\n" \
  "v_cvt_pk_bf16_f32 v14, v28, v29\n" \
  "v_cvt_pk_bf16_f32 v15, v30, v31\n" \
  "global_store_dwordx4 %[ea3], v[12:15], %[wsb] offset:0 sc0 sc1\n" \
  "v_cvt_pk_bf16_f32 v44, v44, v45\n" \
  "v_cvt_pk_bf16_f32 v45, v46, v47\n" \
  "v_cvt_pk_bf16_f32 v46, v60, v61\n" \
  "v_cvt_pk_bf16_f32 v47, v62, v63\n" \
  "global_store_dwordx4 %[ea3], v[44:47], %[wsb] offset:1280 sc0 sc1\n" \
  ""
#define QKV_ASM_PRO \
  "s_mov_b64 s[44:45], %[xg]\n" \
  "s_mov_b64 s[46:47], %[wg]\n" \
  "s_mov_b32 s48, %[rot]\n" \
  "s_add_u32 s40, s44, s48\n" \
  "s_addc_u32 s41, s45, 0\n" \
  "s_mul_i32 s49, s48, 1920\n" \
  "s_add_u32 s42, s46, s49\n" \
  "s_addc_u32 s43, s47, 0\n" \
  "s_add_u32 m0, %[xd], 0\n" \
  "s_nop 0\n" \
  "global_load_lds_dwordx4 %[xs0], s[40:41]\n" \
  "s_add_u32 m0, %[xd], 1024\n" \
  "s_nop 0\n" \
  "global_load_lds_dwordx4 %[xs1], s[40:41]\n" \
  "s_add_u32 m0, %[xd], 2048\n" \
  "s_nop 0\n" \
  "global_load_lds_dwordx4 %[xs2], s[40:41]\n" \
  "s_add_u32 m0, %[xd], 3072\n" \
  "s_nop 0\n" \
  "global_load_lds_dwordx4 %[xs3], s[40:41]\n" \
  "s_add_u32 m0, %[wd], 0\n" \
  "s_nop 0\n" \
  "global_load_lds_dwordx4 %[ws0], s[42:43]\n" \
  "s_add_u32 m0, %[wd], 1024\n" \
  "s_nop 0\n" \
  "global_load_lds_dwordx4 %[ws1], s[42:43]\n" \
  "s_add_u32 s48, s48, 128\n" \
  "s_cmp_eq_u32 s48, 1280\n" \
  "s_cselect_b32 s48, 0, s48\n" \
  "s_add_u32 s40, s44, s48\n" \
  "s_addc_u32 s41, s45, 0\n" \
  "s_mul_i32 s49, s48, 1920\n" \
  "s_add_u32 s42, s46, s49\n" \
  "s_addc_u32 s43, s47, 0\n" \
  ""
#define QKV_PRO_CLOBBERS "s40", "s41", "s42", "s43", "s44", "s45", "s46", "s47", "s48", "s49", "m0", "scc", "memory"
#define QKV_CLOBBERS "v0", "v1", "v2", "v3", "v4", "v5", "v6", "v7", "v8", "v9", "v10", "v11", "v12", "v13", "v14", "v15", "v16", "v17", "v18", "v19", "v20", "v21", "v22", "v23", "v24", "v25", "v26", "v27", "v28", "v29", "v30", "v31", "v32", "v33", "v34", "v35", "v36", "v37", "v38", "v39", "v40", "v41", "v42", "v43", "v44", "v45", "v46", "v47", "v48", "v49", "v50", "v51", "v52", "v53", "v54", "v55", "v56", "v57", "v58", "v59", "v60", "v61", "v62", "v63", "v64", "v65", "v66", "v67", "v68", "v69", "v70", "v71", "v72", "v73", "v74", "v75", "v76", "v77", "v78", "v79", "v80", "v81", "v82", "v83", "v84", "v85", "v86", "v87", "v88", "v89", "v90", "v91", "v92", "v93", "v94", "v95", "v96", "v97", "v98", "v99", "v100", "v101", "v102", "v103", "v104", "v105", "v106", "v107", "v108", "v109", "v110", "v111", "v112", "v113", "v114", "v115", "v116", "v117", "v118", "v119", "v120", "v121", "v122", "v123", "v124", "v125", "v126", "v127", "v128", "v129", "v130", "v131", "v132", "v133", "v134", "v135", "s40", "s41", "s42", "s43", "s44", "s45", "s46", "s47", "s48", "s49", "s50", "s51", "m0", "scc", "memory"

#define OUT2_ASM \
  "s_mov_b64 s[40:41], %[og]\n" \
  "global_load_dwordx4 v[0:3], %[oa], s[40:41] sc1\n" \
  "global_load_dwordx4 v[4:7], %[oa], s[40:41] offset:256 sc1\n" \
  "s_add_u32 s40, s40, 131072\n" \
  "s_addc_u32 s41, s41, 0\n" \
  "global_load_dwordx4 v[8:11], %[oa], s[40:41] sc1\n" \
  "global_load_dwordx4 v[12:15], %[oa], s[40:41] offset:256 sc1\n" \
  "s_add_u32 s40, s40, 131072\n" \
  "s_addc_u32 s41, s41, 0\n" \
  "global_load_dwordx4 v[16:19], %[oa], s[40:41] sc1\n" \
  "global_load_dwordx4 v[20:23], %[oa], s[40:41] offset:256 sc1\n" \
  "s_add_u32 s40, s40, 131072\n" \
  "s_addc_u32 s41, s41, 0\n" \
  "ds_read_b64_tr_b16 v[120:121], %[wa] offset:0\n" \
  "ds_read_b64_tr_b16 v[122:123], %[wa] offset:1280\n" \
  "ds_read_b64_tr_b16 v[124:125], %[wa] offset:32\n" \
  "ds_read_b64_tr_b16 v[126:127], %[wa] offset:1312\n" \
  "ds_read_b64_tr_b16 v[128:129], %[wa] offset:64\n" \
  "ds_read_b64_tr_b16 v[130:131], %[wa] offset:1344\n" \
  "ds_read_b64_tr_b16 v[132:133], %[wa] offset:96\n" \
  "ds_read_b64_tr_b16 v[134:135], %[wa] offset:1376\n" \
  "ds_read_b64_tr_b16 v[136:137], %[wa] offset:128\n" \
  "ds_read_b64_tr_b16 v[138:139], %[wa] offset:1408\n" \
  "s_waitcnt vmcnt(4) lgkmcnt(0)\n" \
  "v_mfma_f32_16x16x32_bf16 v[80:83], v[120:123], v[0:3], 0\n" \
  "ds_read_b64_tr_b16 v[140:141], %[wa] offset:5120\n" \
  "v_mfma_f32_16x16x32_bf16 v[100:103], v[120:123], v[4:7], 0\n" \
  "ds_read_b64_tr_b16 v[142:143], %[wa] offset:6400\n" \
  "global_load_dwordx4 v[24:27], %[oa], s[40:41] sc1\n" \
  "v_mfma_f32_16x16x32_bf16 v[84:87], v[124:127], v[0:3], 0\n" \
  "ds_read_b64_tr_b16 v[144:145], %[wa] offset:5152\n" \
  "v_mfma_f32_16x16x32_bf16 v[104:107], v[124:127], v[4:7], 0\n" \
  "ds_read_b64_tr_b16 v[146:147], %[wa] offset:6432\n" \
  "v_mfma_f32_16x16x32_bf16 v[88:91], v[128:131], v[0:3], 0\n" \
  "ds_read_b64_tr_b16 v[148:149], %[wa] offset:5184\n" \
  "v_mfma_f32_16x16x32_bf16 v[108:111], v[128:131], v[4:7], 0\n" \
  "ds_read_b64_tr_b16 v[150:151], %[wa] offset:6464\n" \
  "global_load_dwordx4 v[28:31], %[oa], s[40:41] offset:256 sc1\n" \
  "s_add_u32 s40, s40, 131072\n" \
  "s_addc_u32 s41, s41, 0\n" \
  "v_mfma_f32_16x16x32_bf16 v[92:95], v[132:135], v[0:3], 0\n" \
  "ds_read_b64_tr_b16 v[152:153], %[wa] offset:5216\n" \
  "v_mfma_f32_16x16x32_bf16 v[112:115], v[132:135], v[4:7], 0\n" \
  "ds_read_b64_tr_b16 v[154:155], %[wa] offset:6496\n" \
  "v_mfma_f32_16x16x32_bf16 v[96:99], v[136:139], v[0:3], 0\n" \
  "ds_read_b64_tr_b16 v[156:157], %[wa] offset:5248\n" \
  "v_mfma_f32_16x16x32_bf16 v[116:119], v[136:139], v[4:7], 0\n" \
  "ds_read_b64_tr_b16 v[158:159], %[wa] offset:6528\n" \
  "s_waitcnt vmcnt(4) lgkmcnt(0)\n" \
  "v_mfma_f32_16x16x32_bf16 v[80:83], v[140:143], v[8:11], v[80:83]\n" \
  "ds_read_b64_tr_b16 v[120:121], %[wa] offset:10240\n" \
  "v_mfma_f32_16x16x32_bf16 v[100:103], v[140:143], v[12:15], v[100:103]\n" \
  "ds_read_b64_tr_b16 v[122:123], %[wa] offset:11520\n" \
  "global_load_dwordx4 v[32:35], %[oa], s[40:41] sc1\n" \
  "v_mfma_f32_16x16x32_bf16 v[84:87], v[144:147], v[8:11], v[84:87]\n" \
  "ds_read_b64_tr_b16 v[124:125], %[wa] offset:10272\n" \
  "v_mfma_f32_16x16x32_bf16 v[104:107], v[144:147], v[12:15], v[104:107]\n" \
  "ds_read_b64_tr_b16 v[126:127], %[wa] offset:11552\n" \
  "v_mfma_f32_16x16x32_bf16 v[88:91], v[148:151], v[8:11], v[88:91]\n" \
  "ds_read_b64_tr_b16 v[128:129], %[wa] offset:10304\n" \
  "v_mfma_f32_16x16x32_bf16 v[108:111], v[148:151], v[12:15], v[108:111]\n" \
  "ds_read_b64_tr_b16 v[130:131], %[wa] offset:11584\n" \
  "global_load_dwordx4 v[36:39], %[oa], s[40:41] offset:256 sc1\n" \
  "s_add_u32 s40, s40, 131072\n" \
  "s_addc_u32 s41, s41, 0\n" \
  "v_mfma_f32_16x16x32_bf16 v[92:95], v[152:155], v[8:11], v[92:95]\n" \
  "ds_read_b64_tr_b16 v[132:133], %[wa] offset:10336\n" \
  "v_mfma_f32_16x16x32_bf16 v[112:115], v[152:155], v[12:15], v[112:115]\n" \
  "ds_read_b64_tr_b16 v[134:135], %[wa] offset:11616\n" \
  "v_mfma_f32_16x16x32_bf16 v[96:99], v[156:159], v[8:11], v[96:99]\n" \
  "ds_read_b64_tr_b16 v[136:137], %[wa] offset:10368\n" \
  "v_mfma_f32_16x16x32_bf16 v[116:119], v[156:159], v[12:15], v[116:119]\n" \
  "ds_read_b64_tr_b16 v[138:139], %[wa] offset:11648\n" \
  "s_waitcnt vmcnt(4) lgkmcnt(0)\n" \
  "v_mfma_f32_16x16x32_bf16 v[80:83], v[120:123], v[16:19], v[80:83]\n" \
  "ds_read_b64_tr_b16 v[140:141], %[wa] offset:15360\n" \
  "v_mfma_f32_16x16x32_bf16 v[100:103], v[120:123], v[20:23], v[100:103]\n" \
  "ds_read_b64_tr_b16 v[142:143], %[wa] offset:16640\n" \
  "global_load_dwordx4 v[40:43], %[oa], s[40:41] sc1\n" \
  "v_mfma_f32_16x16x32_bf16 v[84:87], v[124:127], v[16:19], v[84:87]\n" \
  "ds_read_b64_tr_b16 v[144:145], %[wa] offset:15392\n" \
  "v_mfma_f32_16x16x32_bf16 v[104:107], v[124:127], v[20:23], v[104:107]\n" \
  "ds_read_b64_tr_b16 v[146:147], %[wa] offset:16672\n" \
  "v_mfma_f32_16x16x32_bf16 v[88:91], v[128:131], v[16:19], v[88:91]\n" \
  "ds_read_b64_tr_b16 v[148:149], %[wa] offset:15424\n" \
  "v_mfma_f32_16x16x32_bf16 v[108:111], v[128:131], v[20:23], v[108:111]\n" \
  "ds_read_b64_tr_b16 v[150:151], %[wa] offset:16704\n" \
  "global_load_dwordx4 v[44:47], %[oa], s[40:41] offset:256 sc1\n" \
  "s_add_u32 s40, s40, 131072\n" \
  "s_addc_u32 s41, s41, 0\n" \
  "v_mfma_f32_16x16x32_bf16 v[92:95], v[132:135], v[16:19], v[92:95]\n" \
  "ds_read_b64_tr_b16 v[152:153], %[wa] offset:15456\n" \
  "v_mfma_f32_16x16x32_bf16 v[112:115], v[132:135], v[20:23], v[112:115]\n" \
  "ds_read_b64_tr_b16 v[154:155], %[wa] offset:16736\n" \
  "v_mfma_f32_16x16x32_bf16 v[96:99], v[136:139], v[16:19], v[96:99]\n" \
  "ds_read_b64_tr_b16 v[156:157], %[wa] offset:15488\n" \
  "v_mfma_f32_16x16x32_bf16 v[116:119], v[136:139], v[20:23], v[116:119]\n" \
  "ds_read_b64_tr_b16 v[158:159], %[wa] offset:16768\n" \
  "s_waitcnt vmcnt(4) lgkmcnt(0)\n" \
  "v_mfma_f32_16x16x32_bf16 v[80:83], v[140:143], v[24:27], v[80:83]\n" \
  "ds_read_b64_tr_b16 v[120:121], %[wa] offset:20480\n" \
  "v_mfma_f32_16x16x32_bf16 v[100:103], v[140:143], v[28:31], v[100:103]\n" \
  "ds_read_b64_tr_b16 v[122:123], %[wa] offset:21760\n" \
  "global_load_dwordx4 v[48:51], %[oa], s[40:41] sc1\n" \
  "v_mfma_f32_16x16x32_bf16 v[84:87], v[144:147], v[24:27], v[84:87]\n" \
  "ds_read_b64_tr_b16 v[124:125], %[wa] offset:20512\n" \
  "v_mfma_f32_16x16x32_bf16 v[104:107], v[144:147], v[28:31], v[104:107]\n" \
  "ds_read_b64_tr_b16 v[126:127], %[wa] offset:21792\n" \
  "v_mfma_f32_16x16x32_bf16 v[88:91], v[148:151], v[24:27], v[88:91]\n" \
  "ds_read_b64_tr_b16 v[128:129], %[wa] offset:20544\n" \
  "v_mfma_f32_16x16x32_bf16 v[108:111], v[148:151], v[28:31], v[108:111]\n" \
  "ds_read_b64_tr_b16 v[130:131], %[wa] offset:21824\n" \
  "global_load_dwordx4 v[52:55], %[oa], s[40:41] offset:256 sc1\n" \
  "s_add_u32 s40, s40, 131072\n" \
  "s_addc_u32 s41, s41, 0\n" \
  "v_mfma_f32_16x16x32_bf16 v[92:95], v[152:155], v[24:27], v[92:95]\n" \
  "ds_read_b64_tr_b16 v[132:133], %[wa] offset:20576\n" \
  "v_mfma_f32_16x16x32_bf16 v[112:115], v[152:155], v[28:31], v[112:115]\n" \
  "ds_read_b64_tr_b16 v[134:135], %[wa] offset:21856\n" \
  "v_mfma_f32_16x16x32_bf16 v[96:99], v[156:159], v[24:27], v[96:99]\n" \
  "ds_read_b64_tr_b16 v[136:137], %[wa] offset:20608\n" \
  "v_mfma_f32_16x16x32_bf16 v[116:119], v[156:159], v[28:31], v[116:119]\n" \
  "ds_read_b64_tr_b16 v[138:139], %[wa] offset:21888\n" \
  "s_waitcnt vmcnt(4) lgkmcnt(0)\n" \
  "v_mfma_f32_16x16x32_bf16 v[80:83], v[120:123], v[32:35], v[80:83]\n" \
  "ds_read_b64_tr_b16 v[140:141], %[wa] offset:25600\n" \
  "v_mfma_f32_16x16x32_bf16 v[100:103], v[120:123], v[36:39], v[100:103]\n" \
  "ds_read_b64_tr_b16 v[142:143], %[wa] offset:26880\n" \
  "global_load_dwordx4 v[56:59], %[oa], s[40:41] sc1\n" \
  "v_mfma_f32_16x16x32_bf16 v[84:87], v[124:127], v[32:35], v[84:87]\n" \
  "ds_read_b64_tr_b16 v[144:145], %[wa] offset:25632\n" \
  "v_mfma_f32_16x16x32_bf16 v[104:107], v[124:127], v[36:39], v[104:107]\n" \
  "ds_read_b64_tr_b16 v[146:147], %[wa] offset:26912\n" \
  "v_mfma_f32_16x16x32_bf16 v[88:91], v[128:131], v[32:35], v[88:91]\n" \
  "ds_read_b64_tr_b16 v[148:149], %[wa] offset:25664\n" \
  "v_mfma_f32_16x16x32_bf16 v[108:111], v[128:131], v[36:39], v[108:111]\n" \
  "ds_read_b64_tr_b16 v[150:151], %[wa] offset:26944\n" \
  "global_load_dwordx4 v[60:63], %[oa], s[40:41] offset:256 sc1\n" \
  "s_add_u32 s40, s40, 131072\n" \
  "s_addc_u32 s41, s41, 0\n" \
  "v_mfma_f32_16x16x32_bf16 v[92:95], v[132:135], v[32:35], v[92:95]\n" \
  "ds_read_b64_tr_b16 v[152:153], %[wa] offset:25696\n" \
  "v_mfma_f32_16x16x32_bf16 v[112:115], v[132:135], v[36:39], v[112:115]\n" \
  "ds_read_b64_tr_b16 v[154:155], %[wa] offset:26976\n" \
  "v_mfma_f32_16x16x32_bf16 v[96:99], v[136:139], v[32:35], v[96:99]\n" \
  "ds_read_b64_tr_b16 v[156:157], %[wa] offset:25728\n" \
  "v_mfma_f32_16x16x32_bf16 v[116:119], v[136:139], v[36:39], v[116:119]\n" \
  "ds_read_b64_tr_b16 v[158:159], %[wa] offset:27008\n" \
  "s_waitcnt vmcnt(4) lgkmcnt(0)\n" \
  "v_mfma_f32_16x16x32_bf16 v[80:83], v[140:143], v[40:43], v[80:83]\n" \
  "ds_read_b64_tr_b16 v[120:121], %[wa] offset:30720\n" \
  "v_mfma_f32_16x16x32_bf16 v[100:103], v[140:143], v[44:47], v[100:103]\n" \
  "ds_read_b64_tr_b16 v[122:123], %[wa] offset:32000\n" \
  "global_load_dwordx4 v[64:67], %[oa], s[40:41] sc1\n" \
  "v_mfma_f32_16x16x32_bf16 v[84:87], v[144:147], v[40:43], v[84:87]\n" \
  "ds_read_b64_tr_b16 v[124:125], %[wa] offset:30752\n" \
  "v_mfma_f32_16x16x32_bf16 v[104:107], v[144:147], v[44:47], v[104:107]\n" \
  "ds_read_b64_tr_b16 v[126:127], %[wa] offset:32032\n" \
  "v_mfma_f32_16x16x32_bf16 v[88:91], v[148:151], v[40:43], v[88:91]\n" \
  "ds_read_b64_tr_b16 v[128:129], %[wa] offset:30784\n" \
  "v_mfma_f32_16x16x32_bf16 v[108:111], v[148:151], v[44:47], v[108:111]\n" \
  "ds_read_b64_tr_b16 v[130:131], %[wa] offset:32064\n" \
  "global_load_dwordx4 v[68:71], %[oa], s[40:41] offset:256 sc1\n" \
  "s_add_u32 s40, s40, 131072\n" \
  "s_addc_u32 s41, s41, 0\n" \
  "v_mfma_f32_16x16x32_bf16 v[92:95], v[152:155], v[40:43], v[92:95]\n" \
  "ds_read_b64_tr_b16 v[132:133], %[wa] offset:30816\n" \
  "v_mfma_f32_16x16x32_bf16 v[112:115], v[152:155], v[44:47], v[112:115]\n" \
  "ds_read_b64_tr_b16 v[134:135], %[wa] offset:32096\n" \
  "v_mfma_f32_16x16x32_bf16 v[96:99], v[156:159], v[40:43], v[96:99]\n" \
  "ds_read_b64_tr_b16 v[136:137], %[wa] offset:30848\n" \
  "v_mfma_f32_16x16x32_bf16 v[116:119], v[156:159], v[44:47], v[116:119]\n" \
  "ds_read_b64_tr_b16 v[138:139], %[wa] offset:32128\n" \
  "s_waitcnt vmcnt(4) lgkmcnt(0)\n" \
  "v_mfma_f32_16x16x32_bf16 v[80:83], v[120:123], v[48:51], v[80:83]\n" \
  "ds_read_b64_tr_b16 v[140:141], %[wa] offset:35840\n" \
  "v_mfma_f32_16x16x32_bf16 v[100:103], v[120:123], v[52:55], v[100:103]\n" \
  "ds_read_b64_tr_b16 v[142:143], %[wa] offset:37120\n" \
  "global_load_dwordx4 v[72:75], %[oa], s[40:41] sc1\n" \
  "v_mfma_f32_16x16x32_bf16 v[84:87], v[124:127], v[48:51], v[84:87]\n" \
  "ds_read_b64_tr_b16 v[144:145], %[wa] offset:35872\n" \
  "v_mfma_f32_16x16x32_bf16 v[104:107], v[124:127], v[52:55], v[104:107]\n" \
  "ds_read_b64_tr_b16 v[146:147], %[wa] offset:37152\n" \
  "v_mfma_f32_16x16x32_bf16 v[88:91], v[128:131], v[48:51], v[88:91]\n" \
  "ds_read_b64_tr_b16 v[148:149], %[wa] offset:35904\n" \
  "v_mfma_f32_16x16x32_bf16 v[108:111], v[128:131], v[52:55], v[108:111]\n" \
  "ds_read_b64_tr_b16 v[150:151], %[wa] offset:37184\n" \
  "global_load_dwordx4 v[76:79], %[oa], s[40:41] offset:256 sc1\n" \
  "s_add_u32 s40, s40, 131072\n" \
  "s_addc_u32 s41, s41, 0\n" \
  "v_mfma_f32_16x16x32_bf16 v[92:95], v[132:135], v[48:51], v[92:95]\n" \
  "ds_read_b64_tr_b16 v[152:153], %[wa] offset:35936\n" \
  "v_mfma_f32_16x16x32_bf16 v[112:115], v[132:135], v[52:55], v[112:115]\n" \
  "ds_read_b64_tr_b16 v[154:155], %[wa] offset:37216\n" \
  "v_mfma_f32_16x16x32_bf16 v[96:99], v[136:139], v[48:51], v[96:99]\n" \
  "ds_read_b64_tr_b16 v[156:157], %[wa] offset:35968\n" \
  "v_mfma_f32_16x16x32_bf16 v[116:119], v[136:139], v[52:55], v[116:119]\n" \
  "ds_read_b64_tr_b16 v[158:159], %[wa] offset:37248\n" \
  "s_waitcnt vmcnt(4) lgkmcnt(0)\n" \
  "v_mfma_f32_16x16x32_bf16 v[80:83], v[140:143], v[56:59], v[80:83]\n" \
  "ds_read_b64_tr_b16 v[120:121], %[wa] offset:40960\n" \
  "v_mfma_f32_16x16x32_bf16 v[100:103], v[140:143], v[60:63], v[100:103]\n" \
  "ds_read_b64_tr_b16 v[122:123], %[wa] offset:42240\n" \
  "v_mfma_f32_16x16x32_bf16 v[84:87], v[144:147], v[56:59], v[84:87]\n" \
  "ds_read_b64_tr_b16 v[124:125], %[wa] offset:40992\n" \
  "v_mfma_f32_16x16x32_bf16 v[104:107], v[144:147], v[60:63], v[104:107]\n" \
  "ds_read_b64_tr_b16 v[126:127], %[wa] offset:42272\n" \
  "v_mfma_f32_16x16x32_bf16 v[88:91], v[148:151], v[56:59], v[88:91]\n" \
  "ds_read_b64_tr_b16 v[128:129], %[wa] offset:41024\n" \
  "v_mfma_f32_16x16x32_bf16 v[108:111], v[148:151], v[60:63], v[108:111]\n" \
  "ds_read_b64_tr_b16 v[130:131], %[wa] offset:42304\n" \
  "v_mfma_f32_16x16x32_bf16 v[92:95], v[152:155], v[56:59], v[92:95]\n" \
  "ds_read_b64_tr_b16 v[132:133], %[wa] offset:41056\n" \
  "v_mfma_f32_16x16x32_bf16 v[112:115], v[152:155], v[60:63], v[112:115]\n" \
  "ds_read_b64_tr_b16 v[134:135], %[wa] offset:42336\n" \
  "v_mfma_f32_16x16x32_bf16 v[96:99], v[156:159], v[56:59], v[96:99]\n" \
  "ds_read_b64_tr_b16 v[136:137], %[wa] offset:41088\n" \
  "v_mfma_f32_16x16x32_bf16 v[116:119], v[156:159], v[60:63], v[116:119]\n" \
  "ds_read_b64_tr_b16 v[138:139], %[wa] offset:42368\n" \
  "s_waitcnt vmcnt(2) lgkmcnt(0)\n" \
  "v_mfma_f32_16x16x32_bf16 v[80:83], v[120:123], v[64:67], v[80:83]\n" \
  "ds_read_b64_tr_b16 v[140:141], %[wa] offset:46080\n" \
  "v_mfma_f32_16x16x32_bf16 v[100:103], v[120:123], v[68:71], v[100:103]\n" \
  "ds_read_b64_tr_b16 v[142:143], %[wa] offset:47360\n" \
  "v_mfma_f32_16x16x32_bf16 v[84:87], v[124:127], v[64:67], v[84:87]\n" \
  "ds_read_b64_tr_b16 v[144:145], %[wa] offset:46112\n" \
  "v_mfma_f32_16x16x32_bf16 v[104:107], v[124:127], v[68:71], v[104:107]\n" \
  "ds_read_b64_tr_b16 v[146:147], %[wa] offset:47392\n" \
  "v_mfma_f32_16x16x32_bf16 v[88:91], v[128:131], v[64:67], v[88:91]\n" \
  "ds_read_b64_tr_b16 v[148:149], %[wa] offset:46144\n" \
  "v_mfma_f32_16x16x32_bf16 v[108:111], v[128:131], v[68:71], v[108:111]\n" \
  "ds_read_b64_tr_b16 v[150:151], %[wa] offset:47424\n" \
  "v_mfma_f32_16x16x32_bf16 v[92:95], v[132:135], v[64:67], v[92:95]\n" \
  "ds_read_b64_tr_b16 v[152:153], %[wa] offset:46176\n" \
  "v_mfma_f32_16x16x32_bf16 v[112:115], v[132:135], v[68:71], v[112:115]\n" \
  "ds_read_b64_tr_b16 v[154:155], %[wa] offset:47456\n" \
  "v_mfma_f32_16x16x32_bf16 v[96:99], v[136:139], v[64:67], v[96:99]\n" \
  "ds_read_b64_tr_b16 v[156:157], %[wa] offset:46208\n" \
  "v_mfma_f32_16x16x32_bf16 v[116:119], v[136:139], v[68:71], v[116:119]\n" \
  "ds_read_b64_tr_b16 v[158:159], %[wa] offset:47488\n" \
  "s_waitcnt vmcnt(0) lgkmcnt(0)\n" \
  "v_mfma_f32_16x16x32_bf16 v[80:83], v[140:143], v[72:75], v[80:83]\n" \
  "v_mfma_f32_16x16x32_bf16 v[100:103], v[140:143], v[76:79], v[100:103]\n" \
  "v_mfma_f32_16x16x32_bf16 v[84:87], v[144:147], v[72:75], v[84:87]\n" \
  "v_mfma_f32_16x16x32_bf16 v[104:107], v[144:147], v[76:79], v[104:107]\n" \
  "v_mfma_f32_16x16x32_bf16 v[88:91], v[148:151], v[72:75], v[88:91]\n" \
  "v_mfma_f32_16x16x32_bf16 v[108:111], v[148:151], v[76:79], v[108:111]\n" \
  "v_mfma_f32_16x16x32_bf16 v[92:95], v[152:155], v[72:75], v[92:95]\n" \
  "v_mfma_f32_16x16x32_bf16 v[112:115], v[152:155], v[76:79], v[112:115]\n" \
  "v_mfma_f32_16x16x32_bf16 v[96:99], v[156:159], v[72:75], v[96:99]\n" \
  "v_mfma_f32_16x16x32_bf16 v[116:119], v[156:159], v[76:79], v[116:119]\n" \
  "s_nop 7\n" \
  ""
#define OUT2_CLOBBERS "v0", "v1", "v2", "v3", "v4", "v5", "v6", "v7", "v8", "v9", "v10", "v11", "v12", "v13", "v14", "v15", "v16", "v17", "v18", "v19", "v20", "v21", "v22", "v23", "v24", "v25", "v26", "v27", "v28", "v29", "v30", "v31", "v32", "v33", "v34", "v35", "v36", "v37", "v38", "v39", "v40", "v41", "v42", "v43", "v44", "v45", "v46", "v47", "v48", "v49", "v50", "v51", "v52", "v53", "v54", "v55", "v56", "v57", "v58", "v59", "v60", "v61", "v62", "v63", "v64", "v65", "v66", "v67", "v68", "v69", "v70", "v71", "v72", "v73", "v74", "v75", "v76", "v77", "v78", "v79", "v120", "v121", "v122", "v123", "v124", "v125", "v126", "v127", "v128", "v129", "v130", "v131", "v132", "v133", "v134", "v135", "v136", "v137", "v138", "v139", "v140", "v141", "v142", "v143", "v144", "v145", "v146", "v147", "v148", "v149", "v150", "v151", "v152", "v153", "v154", "v155", "v156", "v157", "v158", "v159", "s40", "s41", "scc", "memory"


__device__ __forceinline__ unsigned pack_bf16(float a, float b) {
  f32x2 v = {a, b};
  bf16x2 r = __builtin_convertvector(v, bf16x2);
  return __builtin_bit_cast(unsigned, r);
}
__device__ __forceinline__ void st16_dev(void* p, u32x4 v) { asm volatile("global_store_dwordx4 %0, %1, off sc1\n s_nop 1" ::"v"(p), "v"(v) : "memory"); }
__device__ __forceinline__ void st8_dev(void* p, u32x2 v) { asm volatile("global_store_dwordx2 %0, %1, off sc1" ::"v"(p), "v"(v) : "memory"); }
typedef __attribute__((address_space(1))) unsigned gu32;
__device__ __forceinline__ unsigned ld_dev(const char* p) { return __hip_atomic_load((gu32*)p, __ATOMIC_RELAXED, __HIP_MEMORY_SCOPE_AGENT); }
__device__ __forceinline__ void st_dev(char* p, unsigned v) { __hip_atomic_store((gu32*)p, v, __ATOMIC_RELAXED, __HIP_MEMORY_SCOPE_AGENT); }

struct QkvTile {
  int b, mt, nt;
  __device__ __forceinline__ QkvTile(int blk) {
    const int xcd = blk & 7, ii = blk >> 3;
    b = xcd >> 2; mt = (xcd & 3) * 2 + (ii & 1); nt = ii >> 1;
  }
};
struct QkvCopy {
  const char *xg, *wg;
  unsigned xs[4], wsr[2], xd, wd, rot;
};
__device__ __forceinline__ void qkv_copy_bases(QkvCopy& c, const unsigned short* xA, const unsigned short* wA, const unsigned short* xB,
                                               const unsigned short* wB, const int blk) {
  const QkvTile t(blk);
  c.xg = (const char*)(t.b ? xB : xA) + (size_t)(t.mt * 256) * (DM * 2);
  c.wg = (const char*)(t.b ? wB : wA) + t.nt * 256;
}
__device__ __forceinline__ void qkv_copy_offsets(QkvCopy& c, const unsigned ldsbase, const int blk, const int w, const int lane) {
  const QkvTile t(blk);
  const int part = t.nt / 5;
#pragma unroll
  for (int i = 0; i < 4; i++) {
    int tok = (part == 2) ? (32 * w + 16 * (i & 1) + 8 * (lane >> 5) + 4 * (i >> 1) + ((lane >> 3) & 3)) : (32 * w + 8 * i + (lane >> 3));
    c.xs[i] = tok * (DM * 2) + (((lane & 7) ^ (4 * (i & 1) + (lane >> 4))) << 4);
  }
#pragma unroll
  for (int i = 0; i < 2; i++) {
    int k = 8 * w + 4 * i + (lane >> 4), cc = lane & 15;
    int seg = (cc >> 1) ^ ((lane >> 4) | ((w & 1) << 2));
    c.wsr[i] = k * 3840 + (seg * 2 + (cc & 1)) * 16;
  }
  c.xd = ldsbase + w * 4096;
  c.wd = ldsbase + 32768 + w * 2048;
  const int rsel = ((((t.mt >> 1) * 5) >> 1) + (t.nt % 5) * 2) % 10;
  c.rot = rsel * 128;
}
__device__ __forceinline__ void qkv_begin(const QkvCopy& c) {
  asm volatile(QKV_ASM_PRO
               :
               : [xs0] "v"(c.xs[0]), [xs1] "v"(c.xs[1]), [xs2] "v"(c.xs[2]), [xs3] "v"(c.xs[3]), [ws0] "v"(c.wsr[0]), [ws1] "v"(c.wsr[1]),
                 [xg] "s"(c.xg), [wg] "s"(c.wg), [xd] "s"(c.xd), [wd] "s"(c.wd), [rot] "s"(c.rot)
               : QKV_PRO_CLOBBERS);
}
__device__ __forceinline__ unsigned qkv_tile(const QkvCopy& c, char* __restrict__ ws, const unsigned ldsbase, const int blk, const int w,
                                             const int lane) {
  const QkvTile t(blk);
  const int b = t.b, mt = t.mt, nt = t.nt;
  const int wm = w >> 1, wn = w & 1;
  const int part = nt / 5, n0 = (nt - part * 5) * 128;
  const int r = lane & 15, quad = lane >> 4;
  const unsigned xa0 = ldsbase + wm * 8192 + r * 128 + ((quad ^ (r >> 1)) << 4);
  const unsigned xa1 = ldsbase + wm * 8192 + r * 128 + (((4 + quad) ^ (r >> 1)) << 4);
  const int q = (lane >> 2) & 3, p = lane & 3;
  const int sw = q | ((quad & 1) << 2);
  const unsigned wb = ldsbase + 32768 + (8 * quad + q) * 256 + 8 * p;
  const unsigned wa0 = wb + (((4 * wn + 0) ^ sw) << 5), wa1 = wb + (((4 * wn + 1) ^ sw) << 5);
  const unsigned wa2 = wb + (((4 * wn + 2) ^ sw) << 5), wa3 = wb + (((4 * wn + 3) ^ sw) << 5);
  const unsigned rot3 = c.rot + 128 >= 1280 ? c.rot + 128 - 1280 : c.rot + 128;
  const unsigned ro = WS_RS + blk * 128;
  const int T0 = mt * 8 + wm * 2;
  unsigned rv, ea[4];
#define QKV_INPUTS                                                                                                       \
    [xa0] "v"(xa0), [xa1] "v"(xa1), [wa0] "v"(wa0), [wa1] "v"(wa1), [wa2] "v"(wa2), [wa3] "v"(wa3), [xs0] "v"(c.xs[0]),    \
    [xs1] "v"(c.xs[1]), [xs2] "v"(c.xs[2]), [xs3] "v"(c.xs[3]), [ws0] "v"(c.wsr[0]), [ws1] "v"(c.wsr[1]),                  \
    [xg] "s"(c.xg), [wg] "s"(c.wg), [xd] "s"(c.xd), [wd] "s"(c.wd), [rot3] "s"(rot3),                                          \
    [ro] "v"(ro), [wsb] "s"(ws), [w] "s"(w), [ea0] "v"(ea[0]), [ea1] "v"(ea[1]), [ea2] "v"(ea[2]), [ea3] "v"(ea[3])
  if (__builtin_expect(part < 2, 1)) {
    const int row = quad & 1;
    const unsigned hstride = part == 0 ? QIMG_HEAD : KVIMG_HEAD;
    const unsigned base = part == 0 ? WS_Q + b * 32 * QIMG_HEAD : WS_KV + b * 32 * KVIMG_HEAD + (T0 >> 2) * 10240;
    const unsigned mainoff = base + (part == 0 ? T0 : (T0 & 3)) * 1024 + (row * 16 + r) * 16;
    const unsigned tailoff = base + (part == 0 ? 65536 + T0 * 256 : 4096 + (T0 & 3) * 256) + (row * 16 + r) * 8;
    unsigned long long em[4];
    const unsigned kc0 = ((n0 + 64 * wn) >> 3) + (quad >> 1), second = hstride - 1536;
#pragma unroll
    for (int j = 0; j < 4; j++) {
      const unsigned kc = kc0 + 2 * j;
      const unsigned pr = __umul24(kc, 205) >> 10;
      const unsigned c5 = kc - __umul24(pr, 5);
      unsigned m = mainoff + c5 * 512, m2 = m + second;
      asm("" : "+v"(m), "+v"(m2));
      m = c5 > 2 ? m2 : m;
      ea[j] = __umul24(pr, 2 * hstride) + (c5 != 2 ? m : tailoff);
      em[j] = __builtin_amdgcn_ballot_w64(c5 != 2);
    }
    const char* ws2 = ws + hstride;
    asm volatile(QKV_ASM_T
                 : [rv] "=&v"(rv)
                 : QKV_INPUTS, [em0] "s"(em[0]), [em1] "s"(em[1]), [em2] "s"(em[2]), [em3] "s"(em[3]), [wsb2] "s"(ws2),
                   [qs] "s"(part ? 1.0f : QSCALE)
                 : QKV_CLOBBERS);
  } else {
    const unsigned base = WS_KV + b * 32 * KVIMG_HEAD + (T0 >> 2) * 10240 + 5120 + (T0 & 3) * 1280 + quad * 320;
#pragma unroll
    for (int j = 0; j < 4; j++) {
      const int f = n0 + 64 * wn + 16 * j + r;
      const int head = (f * 3277) >> 16, d = f - head * 20;
      const int m = (head & 1) ? (d < 4 ? d + 16 : d - 4) : d;
      ea[j] = base + head * KVIMG_HEAD + m * 16;
    }
    asm volatile(QKV_ASM_N : [rv] "=&v"(rv) : QKV_INPUTS : QKV_CLOBBERS);
    asm volatile("L_qkvN_end:");
  }
  return rv;
}

__device__ __forceinline__ void ld_qk(const char* pm, const char* pt, u32x4& m, u32x2& t) {
  asm volatile("global_load_dwordx4 %0, %2, off sc1\n global_load_dwordx2 %1, %3, off sc1\n s_waitcnt vmcnt(0)"
               : "=&v"(m), "=&v"(t) : "v"(pm), "v"(pt) : "memory");
}
__device__ __forceinline__ f32x16 qk_tile(const char* kt_main, const char* kt_tail, int lane, u32x4 qm, u32x2 qt) {
  u32x4 km;
  u32x2 kt;
  ld_qk(kt_main + lane * 16, kt_tail + (lane & 31) * 8, km, kt);
  f32x16 s = {};
  s = __builtin_amdgcn_mfma_f32_32x32x16_bf16(__builtin_bit_cast(bf16x8, km), __builtin_bit_cast(bf16x8, qm), s, 0, 0, 0);
  s = __builtin_amdgcn_mfma_f32_32x32x8bf16_1k(__builtin_bit_cast(s16x4, kt), __builtin_bit_cast(s16x4, qt), s, 0, 0, 0);
  return s;
}

__device__ __forceinline__ f32x16 attn_tile_safe(const char* qb, const char* kv, int qtile, int lane) {
  int h = lane >> 5, r = lane & 31;
  u32x4 qm;
  u32x2 qt;
  ld_qk(qb + qtile * 1024 + lane * 16, qb + 65536 + qtile * 256 + r * 8, qm, qt);
  if (h) { qt[0] = 0; qt[1] = 0; }
  float m = -__builtin_inff();
#pragma unroll 1
  for (int T = 0; T < 64; T++) {
    const char* sl = kv + (T >> 2) * 10240;
    f32x16 s = qk_tile(sl + (T & 3) * 1024, sl + 4096 + (T & 3) * 256, lane, qm, qt);
#pragma unroll
    for (int i = 0; i < 16; i++) m = fmaxf(m, s[i]);
  }
  m = fmaxf(m, __shfl_xor(m, 32));
  f32x16 o = {};
#pragma unroll 1
  for (int T = 0; T < 64; T++) {
    const char* sl = kv + (T >> 2) * 10240;
    f32x16 s = qk_tile(sl + (T & 3) * 1024, sl + 4096 + (T & 3) * 256, lane, qm, qt);
    unsigned p[8];
#pragma unroll
    for (int i = 0; i < 8; i++) p[i] = pack_bf16(__builtin_amdgcn_exp2f(s[2 * i] - m), __builtin_amdgcn_exp2f(s[2 * i + 1] - m));
    const char* vb = sl + 5120 + (T & 3) * 1280 + (r < 20 ? r : 0) * 16 + h * 8;
    u32x2 v0, v1, v2, v3;
    asm volatile("global_load_dwordx2 %0, %4, off sc1\n global_load_dwordx2 %1, %4, off offset:320 sc1\n"
                 "global_load_dwordx2 %2, %4, off offset:640 sc1\n global_load_dwordx2 %3, %4, off offset:960 sc1\n s_waitcnt vmcnt(0)"
                 : "=&v"(v0), "=&v"(v1), "=&v"(v2), "=&v"(v3) : "v"(vb) : "memory");
#pragma unroll
    for (int c = 0; c < 2; c++) {
      const u32x2 lo = c ? v2 : v0, hi = c ? v3 : v1;
      u32x4 va = {lo[0], lo[1], hi[0], hi[1]};
      if (r >= 20) va = u32x4{0x3f803f80u, 0x3f803f80u, 0x3f803f80u, 0x3f803f80u};
      u32x4 pb = {p[4 * c], p[4 * c + 1], p[4 * c + 2], p[4 * c + 3]};
      o = __builtin_amdgcn_mfma_f32_32x32x16_bf16(__builtin_bit_cast(bf16x8, va), __builtin_bit_cast(bf16x8, pb), o, 0, 0, 0);
    }
  }
  return o;
}

__device__ __forceinline__ void attn_finish(const f32x16& o, char* ob, int head, int token, int h) {
  float l = o[12];
  float inv = __builtin_amdgcn_rcpf(l);
  float r[12];
#pragma unroll
  for (int i = 0; i < 12; i++) r[i] = o[i] * inv;
  int j = head >> 1, odd = head & 1;
  char* p = ob + token * 16;
  u32x2 v0 = {pack_bf16(r[0], r[1]), pack_bf16(r[2], r[3])};
  u32x2 v1 = {pack_bf16(r[4], r[5]), pack_bf16(r[6], r[7])};
  u32x2 v2 = {pack_bf16(r[8], r[9]), pack_bf16(r[10], r[11])};
  st8_dev(p + (5 * j + 3 * odd) * 32768 + h * 8, v0);
  st8_dev(p + (5 * j + 3 * odd + 1) * 32768 + h * 8, v1);
  if (h == 0) st8_dev(p + (5 * j + 2) * 32768 + odd * 8, v2);
}

__device__ __forceinline__ bool attn_finish2(const f32x16& A, const f32x16& B, char* ob, int head, int token) {
  float x[12], y[12];
#pragma unroll
  for (int i = 0; i < 3; i++) {
#pragma unroll
    for (int k = 0; k < 4; k++) {
      auto s = __builtin_amdgcn_permlane32_swap(__float_as_uint(A[4 * i + k]), __float_as_uint(B[4 * i + k]), false, false);
      x[4 * i + k] = __uint_as_float(s[0]);
      y[4 * i + k] = __uint_as_float(s[1]);
    }
  }
  const float l = y[8];
  const float inv = __builtin_amdgcn_rcpf(l);
  unsigned c[10];
  f32x2 sq = {0.f, 0.f};
  const f32x2 inv2 = {inv, inv};
#pragma unroll
  for (int i = 0; i < 3; i++) {
    f32x2 a01 = f32x2{x[4 * i], x[4 * i + 1]} * inv2, a23 = f32x2{x[4 * i + 2], x[4 * i + 3]} * inv2;
    sq = __builtin_elementwise_fma(a01, a01, sq);
    sq = __builtin_elementwise_fma(a23, a23, sq);
    c[4 * i] = pack_bf16(a01[0], a01[1]);
    c[4 * i + 1] = pack_bf16(a23[0], a23[1]);
    if (i < 2) {
      f32x2 b01 = f32x2{y[4 * i], y[4 * i + 1]} * inv2, b23 = f32x2{y[4 * i + 2], y[4 * i + 3]} * inv2;
      sq = __builtin_elementwise_fma(b01, b01, sq);
      sq = __builtin_elementwise_fma(b23, b23, sq);
      c[4 * i + 2] = pack_bf16(b01[0], b01[1]);
      c[4 * i + 3] = pack_bf16(b23[0], b23[1]);
    }
  }
  const bool ok = (l >= 0x1p-64f) && (l <= 0x1p64f) && (sq[0] + sq[1] <= 3.0e38f);
  if (__builtin_amdgcn_ballot_w64(!ok) != 0) return false;
  const int j = head >> 1, odd = head & 1;
  char* p = ob + token * 16;
  st16_dev(p + (5 * j + 3 * odd) * 32768, u32x4{c[0], c[1], c[2], c[3]});
  st16_dev(p + (5 * j + 3 * odd + 1) * 32768, u32x4{c[4], c[5], c[6], c[7]});
  st8_dev(p + (5 * j + 2) * 32768 + odd * 8, u32x2{c[8], c[9]});
  return true;
}

__global__ __launch_bounds__(512, 2) void mha_kernel(const unsigned short* __restrict__ xA, const unsigned short* __restrict__ wA,
                                                     const unsigned short* __restrict__ woA, const unsigned short* __restrict__ xB,
                                                     const unsigned short* __restrict__ wB, const unsigned short* __restrict__ woB,
                                                     unsigned short* __restrict__ out, char* __restrict__ ws) {
  __shared__ __attribute__((aligned(16))) char smem[163840];
  const int blk0 = blockIdx.x;
  const int w0 = __builtin_amdgcn_readfirstlane(threadIdx.x >> 6);
  const int lane0 = threadIdx.x & 63;
  const unsigned ldsbase = (unsigned)(size_t)(__attribute__((address_space(3))) char*)smem;
  if ((blk0 & ~7) == 240) {
    unsigned off = threadIdx.x * 128;
    asm volatile("s_getpc_b64 s[40:41]\n L_epf_%=:\n v_min_u32 %0, L_qkvN_end-L_epf_%=, %0\n global_load_dword %0, %0, s[40:41]\n s_waitcnt vmcnt(0)"
                 : "+v"(off) :: "s40", "s41", "memory");
  }
  typedef unsigned long long u64x8 __attribute__((ext_vector_type(8)));
  const u64x8 karg = *(const volatile __attribute__((address_space(4))) u64x8*)__builtin_amdgcn_kernarg_segment_ptr();
  __builtin_amdgcn_sched_barrier(0);
  QkvCopy qc;
  qkv_copy_offsets(qc, ldsbase, blk0, w0, lane0);
  asm volatile("" : "+v"(qc.xs[0]), "+v"(qc.xs[1]), "+v"(qc.xs[2]), "+v"(qc.xs[3]), "+v"(qc.wsr[0]), "+v"(qc.wsr[1]));
  asm volatile("" : "+s"(qc.xd), "+s"(qc.wd), "+s"(qc.rot));
  __builtin_amdgcn_sched_barrier(0);
#define GPTR(T, x) ((T*)(__attribute__((address_space(1))) T*)(x))
  xA = GPTR(const unsigned short, karg[0]); wA = GPTR(const unsigned short, karg[1]); woA = GPTR(const unsigned short, karg[2]);
  xB = GPTR(const unsigned short, karg[3]); wB = GPTR(const unsigned short, karg[4]); woB = GPTR(const unsigned short, karg[5]);
  out = GPTR(unsigned short, karg[6]); ws = GPTR(char, karg[7]);
  if (blk0 == LEADER) {
    const int t = threadIdx.x;
    if (t < 8 * CREP) st_dev(ws + WS_CNT + t * CSTR, 0);
    if (t < 480) st_dev(ws + WS_QC + t * 128, 0);
    if (t < 256) st_dev(ws + WS_RS + t * 128 + 4, 0);
  }
  qkv_copy_bases(qc, xA, wA, xB, wB, blk0);
  if (blk0 < 240) qkv_begin(qc);
  int blk = blk0, w = w0, lane = lane0;
  asm volatile("" : "+s"(blk), "+s"(w), "+v"(lane));
  char* const slot = ws + WS_RS + blk * 128;
  if (blk != LEADER && w == POLLW && lane == 0) st_dev(slot, 0);
  unsigned rv = 0;
  if (blk < 240) rv = qkv_tile(qc, ws, ldsbase, blk, w, lane);
  asm volatile("" : "+s"(blk));
  asm volatile("" : "+s"(w));
  asm volatile("" : "+v"(lane));
  blk = __builtin_amdgcn_readfirstlane(blk);
  w = __builtin_amdgcn_readfirstlane(w);
  const int xcd = blk & 7, idx = blk >> 3;
  const int pair = xcd * 4 + (idx >> 3);
  const int b = pair >> 4;
  const int head = 2 * (pair & 15) + (idx & 1), quarter = (idx >> 1) & 3;
  const int qg = w & 3, hf = w >> 2;
  const char* kv = ws + WS_KV + (size_t)(b * 32 + head) * KVIMG_HEAD;
  const char* qb = ws + WS_Q + (size_t)(b * 32 + head) * QIMG_HEAD;
  const int tile0 = quarter * 16 + qg * 4;
  const char* wo = (const char*)(b ? woB : woA);
  unsigned pfo = (w * 64 + lane) * 128;
  const int nt = head & 7, mt = quarter * 4 + (head >> 3);
  unsigned sy, tg;
  int role = 0;
  {
    const int pb = (blk & 7) >> 2, pmt = (blk & 3) * 2 + (idx & 1), pnt = idx >> 1, ppart = pnt / 5;
    const int k = (lane - 8) & 7, kind = k >> 1;
    const int ntl = (20 * head + ((k & 1) ? 19 : 0)) >> 7;
    const int lp = ((b * 5 + ntl) * 6 + (kind < 2 ? kind : 2 + quarter)) * 8 + (kind < 2 ? (head & 1) * 4 + quarter : head & 7);
    const int la = ((pb * 5 + pnt - 5 * ppart) * 6 + (ppart == 0 ? 2 + (pmt >> 1) : ppart - 1)) * 8 + (lane & 7);
    sy = WS_QC + (lane < 8 ? la : lp) * 128;
    tg = kind < 2 ? 8 : 2;
    if (w == POLLW) role = blk < 240 ? 2 : 1;
    if (blk == LEADER && w < 4) {
      role = 3;
      sy = WS_RS + (w * 64 + lane) * 128;
      tg = w * 64 + lane != LEADER;
    }
  }
  unsigned pto;
  {
    const int g = lane >> 4;
    pto = WS_O + b * OIMG_B + (5 * (head >> 1) + (g < 2 ? 3 * (head & 1) + g : 2)) * 32768 + tile0 * 512 + (lane & 15) * 128;
    if (g == 3) pto = WS_CNT + ((b * 4 + quarter) * CREP + (head & (CREP - 1))) * CSTR;
  }
  const char* a_img = kv + hf * 81920;
  const char* a_q = qb + tile0 * 1024;
  const char* a_qt = qb + 65536 + tile0 * 256;
  const char* a_wo = wo + nt * 160 + qg * 40960;
  unsigned a_lds = ldsbase + hf * 81920;
  int a_qg = qg, a_hf = hf;
  asm volatile("" : "+s"(a_img), "+s"(a_q), "+s"(a_qt), "+s"(a_wo), "+s"(a_lds), "+s"(a_qg), "+s"(a_hf), "+s"(role), "+v"(pfo), "+v"(pto),
               "+v"(sy), "+v"(tg));
  if (w == POLLW && blk != LEADER && __builtin_amdgcn_readfirstlane(rv) != 1) {
    asm volatile("s_waitcnt vmcnt(0)" ::: "memory");
#pragma unroll 1
    for (unsigned spins = 0; spins < SPIN_MAX; spins++)
      if (__builtin_amdgcn_readfirstlane(ld_dev(slot)) == 1) break;
    if (lane == 0) st_dev(slot + 4, 1);
  }
  f32x16 o0, o1, o2, o3;
  asm volatile(ATTN_ASM_A
               : "=&{v[64:79]}"(o0), "=&{v[80:95]}"(o1), "=&{v[96:111]}"(o2), "=&{v[112:127]}"(o3)
               : [img] "s"(a_img), [q] "s"(a_q), [qt] "s"(a_qt), [lds] "s"(a_lds), [qg] "s"(a_qg), [hf] "s"(a_hf), [wo2] "s"(a_wo),
                 [pfo] "v"(pfo), [pto] "v"(pto), [wsb] "s"(ws), [sy] "v"(sy), [tg] "v"(tg), [role] "s"(role)
               : ATTN_CLOBBERS);
  asm volatile("v_mbcnt_lo_u32_b32 %0, -1, 0\n v_mbcnt_hi_u32_b32 %0, -1, %0" : "=v"(lane));
#define XSLOT(v) ((v) < 4 ? 51200 + (v) * 6144 : (v) < 7 ? 133120 + ((v) - 4) * 6144 : 75776)
#define WOKH 81920
  char* ob = ws + WS_O + (size_t)b * OIMG_B;
  if (hf) {
    const bool ok0 = attn_finish2(o0, o1, ob, head, tile0 * 32 + lane);
    const bool ok1 = attn_finish2(o2, o3, ob, head, (tile0 + 2) * 32 + lane);
    if (!(ok0 && ok1)) {
#pragma unroll 1
      for (int i = 0; i < 4; i++) {
        if (i < 2 ? ok0 : ok1) continue;
        f32x16 o = attn_tile_safe(qb, kv, tile0 + i, lane);
        attn_finish(o, ob, head, (tile0 + i) * 32 + (lane & 31), lane >> 5);
      }
    }
  }
  asm volatile("s_waitcnt vmcnt(0)" ::: "memory");
  asm volatile("s_barrier" ::: "memory");
  if (w == POLLW) {
    char* cb = ws + WS_CNT + (b * 4 + quarter) * (CREP * CSTR);
    if (lane < CREP) __hip_atomic_fetch_add((gu32*)(cb + lane * CSTR), 1u, __ATOMIC_RELAXED, __HIP_MEMORY_SCOPE_AGENT);
    gu32* cnt = (gu32*)(cb + (head & (CREP - 1)) * CSTR);
    for (unsigned spins = 0; spins < SPIN_MAX; spins++) {
      unsigned v = __builtin_amdgcn_readfirstlane(__hip_atomic_load(cnt, __ATOMIC_RELAXED, __HIP_MEMORY_SCOPE_AGENT));
      if (v >= 32) break;
    }
  }
  asm volatile("s_barrier" ::: "memory");
  const int tg4 = w & 3, kh = w >> 2;
  const int r = lane & 15, quad = lane >> 4;
  const char* og = ws + WS_O + (size_t)b * OIMG_B + kh * (40 * 32768) + (mt * 128 + tg4 * 32) * 16;
  const unsigned oa = quad * 32768 + r * 16;
  const unsigned wa = ldsbase + kh * WOKH + (16 * (quad >> 1) + 4 * (quad & 1) + ((lane >> 2) & 3)) * 160 + 8 * (lane & 3);
  f32x16 a0, a1;
  f32x8 a2;
  asm volatile(OUT2_ASM
               : "=&{v[80:95]}"(a0), "=&{v[96:111]}"(a1), "=&{v[112:119]}"(a2)
               : [wa] "v"(wa), [oa] "v"(oa), [og] "s"(og)
               : OUT2_CLOBBERS);
  float acc[40];
#pragma unroll
  for (int i = 0; i < 16; i++) { acc[i] = a0[i]; acc[16 + i] = a1[i]; }
#pragma unroll
  for (int i = 0; i < 8; i++) acc[32 + i] = a2[i];
  f32x4* ey = (f32x4*)(smem + XSLOT(w)) + lane;
  f32x4 keep[5];
#pragma unroll
  for (int j = 0; j < 5; j++) {
    f32x4 lo = {acc[4 * j], acc[4 * j + 1], acc[4 * j + 2], acc[4 * j + 3]};
    f32x4 hi = {acc[20 + 4 * j], acc[20 + 4 * j + 1], acc[20 + 4 * j + 2], acc[20 + 4 * j + 3]};
    keep[j] = kh ? hi : lo;
    ey[j * 64] = kh ? lo : hi;
  }
  __syncthreads();
  const f32x4* ry = (const f32x4*)(smem + XSLOT(w ^ 4)) + lane;
  unsigned pk[10];
#pragma unroll
  for (int j = 0; j < 5; j++) {
    f32x4 v = keep[j] + ry[j * 64];
    pk[2 * j] = pack_bf16(v[0], v[1]);
    pk[2 * j + 1] = pack_bf16(v[2], v[3]);
  }
  char* op = (char*)out + (size_t)(mt * 128 + tg4 * 32 + kh * 16 + r) * 2560 + b * 1280 + nt * 160;
#pragma unroll
  for (int jp = 0; jp < 2; jp++) {
    auto s0 = __builtin_amdgcn_permlane16_swap(pk[4 * jp], pk[4 * jp + 2], false, false);
    auto s1 = __builtin_amdgcn_permlane16_swap(pk[4 * jp + 1], pk[4 * jp + 3], false, false);
    u32x4 v = {s0[0], s1[0], s0[1], s1[1]};
    *(u32x4*)(op + jp * 64 + (quad & 1) * 32 + (quad >> 1) * 16) = v;
  }
  *(u32x2*)(op + 128 + quad * 8) = u32x2{pk[8], pk[9]};
  asm volatile("L_attn_end:");
}

extern "C" void kernel_launch(void* const* d_in, const int* in_sizes, int n_in, void* d_out, int out_size,
                              void* d_ws, size_t ws_size, hipStream_t stream) {
  mha_kernel<<<256, 512, 0, stream>>>((const unsigned short*)d_in[0], (const unsigned short*)d_in[1], (const unsigned short*)d_in[2],
                                      (const unsigned short*)d_in[3], (const unsigned short*)d_in[4], (const unsigned short*)d_in[5],
                                      (unsigned short*)d_out, (char*)d_ws);
}
```
